```python
import torch

def _softmax(x, axis=-1):
    return torch.softmax(x, dim=axis)

def _gelu(x):
    return torch.nn.functional.gelu(x, approximate='tanh')

def _silu(x):
    return torch.nn.functional.silu(x)

def _sigmoid(x):
    return torch.sigmoid(x)

def _layernorm(x, axis=-1, eps=1e-05):
    m = x.mean(dim=axis, keepdim=True)
    v = x.var(dim=axis, keepdim=True, unbiased=False)
    return (x - m) / torch.sqrt(v + eps)

def _scatter_add(tgt, idx, src, axis=0):
    return tgt.clone().index_add_(axis, idx, src)

def _segment_sum(src, seg, n):
    out = torch.zeros((n,) + src.shape[1:], dtype=src.dtype)
    return out.index_add_(0, seg, src)

def _pad(x, pw):
    flat = tuple((v for pair in reversed(pw) for v in pair))
    return torch.nn.functional.pad(x, flat)
kernel_name = 'interleave_layers__ssm_conv_h3__mixer_gmlp__prompt'

def setup_inputs():

    _w = lambda a: (lambda t: t.to(torch.bfloat16) if t.is_floating_point() else t)(torch.as_tensor(a))

    def _A_setup():
        B, S, D = (1, 2048, 640)
        ks = 4
        return {'q': _w((lambda *s: torch.randn(*s, dtype=torch.bfloat16) * 0.05)(B, S, D)), 'k': _w((lambda *s: torch.randn(*s, dtype=torch.bfloat16) * 0.8)(B, S, D)), 'v': _w((lambda *s: torch.randn(*s, dtype=torch.bfloat16) * 0.8)(B, S, D)), 'shift_k': _w((lambda *s: torch.randn(*s, dtype=torch.bfloat16) * 0.8)(D, ks) * 0.5), 'ssm_k': _w((lambda *s: torch.randn(*s, dtype=torch.bfloat16) * 0.8)(D, S) * (1.0 / S ** 0.5)), 'ssm_D': _w((lambda *s: torch.randn(*s, dtype=torch.bfloat16) * 0.8)(D))}
    _A_in = _A_setup()

    def _B_setup():
        B, S, D, F = (1, 2048, 640, 1536)
        Fh = F // 2
        return {'x': _w((lambda *s: torch.randn(*s, dtype=torch.bfloat16) * 0.05)(B, S, D)), 'w_in': _w((lambda *s: torch.randn(*s, dtype=torch.bfloat16) * 0.4)(D, 2 * Fh)), 'w_sp': _w(0.01 * (lambda *s: torch.randn(*s, dtype=torch.bfloat16) * 0.4)(S, S)), 'b_sp': _w(1.0 + 0.01 * (lambda *s: torch.randn(*s, dtype=torch.bfloat16) * 0.1)(S)), 'w_out': _w((lambda *s: torch.randn(*s, dtype=torch.bfloat16) * 0.1)(Fh, D))}
    _B_in = _B_setup()
    return {'A_q': _A_in['q'], 'A_k': _A_in['k'], 'A_v': _A_in['v'], 'A_shift_k': _A_in['shift_k'], 'A_ssm_k': _A_in['ssm_k'], 'A_ssm_D': _A_in['ssm_D'], 'B_x': _B_in['x'], 'B_w_in': _B_in['w_in'], 'B_w_sp': _B_in['w_sp'], 'B_b_sp': _B_in['b_sp'], 'B_w_out': _B_in['w_out']}

def reference(A_q, A_k, A_v, A_shift_k, A_ssm_k, A_ssm_D, B_x, B_w_in, B_w_sp, B_b_sp, B_w_out):

    def _A_ref(q, k, v, shift_k, ssm_k, ssm_D):
        B, S, D = q.shape
        ks = shift_k.shape[-1]
        k_pad = _pad(k, ((0, 0), (ks - 1, 0), (0, 0)))
        k_shift = torch.zeros_like(k)
        for t in range(ks):
            k_shift = k_shift + k_pad[:, t:t + S, :] * shift_k[:, ks - 1 - t][None, None, :]
        kv = k_shift * v
        L = 2 * S
        kv_pad = _pad(kv, ((0, 0), (0, S), (0, 0)))
        kf_pad = _pad(ssm_k, ((0, 0), (0, S)))
        kv_f = (lambda x, axes=(-2, -1): torch.fft.rfft2(x.float(), dim=axes))(kv_pad[:, None, :, :], axes=(1, 2))
        kf_f = (lambda x, axes=(-2, -1): torch.fft.rfft2(x.float(), dim=axes))(kf_pad[:, None, :], axes=(1, 2))
        kf_f = (lambda x, *d: x.permute(*d))(kf_f, 1, 2, 0)
        y_f = kv_f * kf_f[None, :, :, :]
        y = (lambda x, s, axes=(-2, -1): torch.fft.irfft2(x, s=s, dim=axes).to(torch.bfloat16))(y_f, (1, L), axes=(1, 2))[:, 0, :S, :]
        y = y + kv * ssm_D[None, None, :]
        return q * y

    def _B_ref(x, w_in, w_sp, b_sp, w_out):
        B, S, D = x.shape
        F = w_in.shape[-1]
        Fh = F // 2
        z = _gelu(torch.einsum('bsd,df->bsf', x, w_in))
        z1 = z[..., :Fh].clone()
        z2 = _layernorm(z[..., Fh:], axis=-1)
        g = torch.einsum('ts,bsf->btf', w_sp, z2) + b_sp[None, :, None]
        return torch.einsum('bsf,fd->bsd', z1 * g, w_out)
    x = A_q
    x = x + _A_ref(_layernorm(x, axis=-1), A_k, A_v, A_shift_k, A_ssm_k, A_ssm_D)
    x = x + _B_ref(_layernorm(x, axis=-1), B_w_in, B_w_sp, B_b_sp, B_w_out)
    x = x + _A_ref(_layernorm(x, axis=-1), A_k, A_v, A_shift_k, A_ssm_k, A_ssm_D)
    x = x + _B_ref(_layernorm(x, axis=-1), B_w_in, B_w_sp, B_b_sp, B_w_out)
    return x
```

```cpp
#include <hip/hip_runtime.h>
#include <hip/hip_bf16.h>
#include <stdint.h>

typedef unsigned short u16;
constexpr int S = 2048, D = 640, F = 1536, FH = 768;

__device__ __forceinline__ float bf2f(u16 b) { return __uint_as_float(((unsigned)b) << 16); }
__device__ __forceinline__ u16 f2bf(float f) {
    __hip_bfloat16 h = __float2bfloat16(f); return *reinterpret_cast<u16*>(&h);
}
__device__ __forceinline__ float rbf(float f) { return bf2f(f2bf(f)); }

__global__ void k_kv(const u16* k, const u16* v, const u16* shk, u16* kvT) {
    int idx = blockIdx.x * 256 + threadIdx.x;
    if (idx >= S * D) return;
    int s = idx / D, d = idx % D;
    float acc = 0.f;
    for (int t = 0; t < 4; ++t) {
        int ss = s + t - 3;
        float kk = ss >= 0 ? bf2f(k[ss * D + d]) : 0.f;
        float p = rbf(kk * bf2f(shk[d * 4 + 3 - t]));
        acc = rbf(acc + p);
    }
    kvT[d * S + s] = f2bf(acc * bf2f(v[idx]));
}
__global__ void k_conv(const u16* kvT, const u16* ssmk, const u16* ssmD, u16* y) {
    __shared__ float a[S], b[S];
    int d = blockIdx.x;
    for (int i = threadIdx.x; i < S; i += 256) { a[i] = bf2f(kvT[d * S + i]); b[i] = bf2f(ssmk[d * S + i]); }
    __syncthreads();
    float Dd = bf2f(ssmD[d]);
    for (int s = threadIdx.x; s < S; s += 256) {
        float acc = 0.f;
        for (int t = 0; t <= s; ++t) acc += a[t] * b[s - t];
        float yy = rbf(acc);
        yy = rbf(yy + rbf(a[s] * Dd));
        y[s * D + d] = f2bf(yy);
    }
}
__device__ float block_sum(float v, float* red) {
    for (int o = 32; o; o >>= 1) v += __shfl_xor(v, o);
    __syncthreads();
    if ((threadIdx.x & 63) == 0) red[threadIdx.x >> 6] = v;
    __syncthreads();
    float r = 0.f;
    for (int i = 0; i < (int)(blockDim.x >> 6); ++i) r += red[i];
    return r;
}
__device__ void ln_row(float* x, int n, float* red) {
    float s = 0.f;
    for (int i = threadIdx.x; i < n; i += blockDim.x) s += x[i];
    float mean = block_sum(s, red) / n;
    float q = 0.f;
    for (int i = threadIdx.x; i < n; i += blockDim.x) { float dd = x[i] - mean; q += dd * dd; }
    float var = block_sum(q, red) / n;
    float m = rbf(mean), vv = rbf(var);
    float sd = rbf(sqrtf(rbf(vv + 1e-5f)));
    for (int i = threadIdx.x; i < n; i += blockDim.x) x[i] = rbf(rbf(x[i] - m) / sd);
    __syncthreads();
}
__global__ void k_a(const u16* x, const u16* y, u16* xout, u16* h) {
    __shared__ float t[D], xs[D], red[8];
    int s = blockIdx.x;
    for (int i = threadIdx.x; i < D; i += blockDim.x) { xs[i] = t[i] = bf2f(x[s * D + i]); }
    __syncthreads();
    ln_row(t, D, red);
    for (int i = threadIdx.x; i < D; i += blockDim.x) {
        float v = rbf(xs[i] + rbf(t[i] * bf2f(y[s * D + i])));
        xout[s * D + i] = f2bf(v); t[i] = v;
    }
    __syncthreads();
    ln_row(t, D, red);
    for (int i = threadIdx.x; i < D; i += blockDim.x) h[s * D + i] = f2bf(t[i]);
}
__global__ void k_lnz(u16* z) {
    __shared__ float t[FH], red[8];
    int s = blockIdx.x;
    for (int i = threadIdx.x; i < FH; i += blockDim.x) t[i] = bf2f(z[s * F + FH + i]);
    __syncthreads();
    ln_row(t, FH, red);
    for (int i = threadIdx.x; i < FH; i += blockDim.x) z[s * F + FH + i] = f2bf(t[i]);
}
__device__ __forceinline__ float gelu(float x) {
    float u = 0.7978845608028654f * (x + 0.044715f * x * x * x);
    return 0.5f * x * (1.f + tanhf(u));
}
template <int MODE>
__global__ void k_gemm(const u16* A, int lda, const u16* B, int ldb, u16* C, int ldc, int K,
                       const u16* aux, int ldaux, const u16* bias) {
    __shared__ float As[32][33], Bs[32][33];
    int tx = threadIdx.x & 31, ty = threadIdx.x >> 5;
    int m0 = blockIdx.y * 32, n0 = blockIdx.x * 32;
    float acc[4] = {0, 0, 0, 0};
    for (int k0 = 0; k0 < K; k0 += 32) {
        for (int r = 0; r < 4; ++r) {
            As[ty + 8 * r][tx] = bf2f(A[(m0 + ty + 8 * r) * lda + k0 + tx]);
            Bs[ty + 8 * r][tx] = bf2f(B[(k0 + ty + 8 * r) * ldb + n0 + tx]);
        }
        __syncthreads();
        for (int kk = 0; kk < 32; ++kk)
            for (int r = 0; r < 4; ++r) acc[r] += As[ty + 8 * r][kk] * Bs[kk][tx];
        __syncthreads();
    }
    for (int r = 0; r < 4; ++r) {
        int m = m0 + ty + 8 * r, n = n0 + tx;
        float v = rbf(acc[r]);
        if (MODE == 0) v = gelu(v);
        if (MODE == 1) { v = rbf(v + bf2f(bias[m])); v = bf2f(aux[m * ldaux + n]) * v; }
        if (MODE == 2) { v = bf2f(aux[m * ldaux + n]) + v; }
        C[m * ldc + n] = f2bf(v);
    }
}

extern "C" void kernel_launch(void* const* d_in, const int* in_sizes, int n_in,
                              void* d_out, int out_size, void* d_ws, size_t ws_size,
                              hipStream_t stream) {
    const u16* q = (const u16*)d_in[0];
    const u16* k = (const u16*)d_in[1];
    const u16* v = (const u16*)d_in[2];
    const u16* shk = (const u16*)d_in[3];
    const u16* ssmk = (const u16*)d_in[4];
    const u16* ssmD = (const u16*)d_in[5];
    const u16* w_in = (const u16*)d_in[7];
    const u16* w_sp = (const u16*)d_in[8];
    const u16* b_sp = (const u16*)d_in[9];
    const u16* w_out = (const u16*)d_in[10];
    u16* out = (u16*)d_out;
    u16* ws = (u16*)d_ws;
    u16* kvT = ws; u16* y = kvT + S * D; u16* x1 = y + S * D; u16* h = x1 + S * D;
    u16* z = h + S * D; u16* u = z + S * F; u16* x2 = u + S * FH;
    k_kv<<<S * D / 256, 256, 0, stream>>>(k, v, shk, kvT);
    k_conv<<<D, 256, 0, stream>>>(kvT, ssmk, ssmD, y);
    const u16* xin = q;
    for (int l = 0; l < 2; ++l) {
        k_a<<<S, 256, 0, stream>>>(xin, y, x1, h);
        k_gemm<0><<<dim3(F / 32, S / 32), 256, 0, stream>>>(h, D, w_in, F, z, F, D, nullptr, 0, nullptr);
        k_lnz<<<S, 256, 0, stream>>>(z);
        k_gemm<1><<<dim3(FH / 32, S / 32), 256, 0, stream>>>(w_sp, S, z + FH, F, u, FH, S, z, F, b_sp);
        k_gemm<2><<<dim3(D / 32, S / 32), 256, 0, stream>>>(u, FH, w_out, D, l == 0 ? x2 : out, D, FH, x1, D, nullptr);
        xin = x2;
    }
}
```

```cpp
#include <hip/hip_runtime.h>
#include <stdint.h>

typedef unsigned short u16;
typedef unsigned v4u __attribute__((ext_vector_type(4)));
typedef unsigned v2u __attribute__((ext_vector_type(2)));
typedef float f32x4 __attribute__((ext_vector_type(4)));
typedef float f32x2 __attribute__((ext_vector_type(2)));
typedef short s16x4 __attribute__((ext_vector_type(4)));
typedef __bf16 bf2v __attribute__((ext_vector_type(2)));
#define GAS __attribute__((address_space(1)))
#define LAS __attribute__((address_space(3)))
typedef GAS unsigned gu32;
#define RLX_AGENT __ATOMIC_RELAXED, __HIP_MEMORY_SCOPE_AGENT
#define DRAIN() asm volatile("s_waitcnt vmcnt(0)" ::: "memory")
#define WAITV(n) asm volatile("s_waitcnt vmcnt(%0)" :: "i"(n) : "memory")
#define CBAR() asm volatile("" ::: "memory")
#define DI __device__ __forceinline__
#define SB() __builtin_amdgcn_sched_barrier(0)
#define OPQ(x) asm volatile("" : "+s"(x))
#define BAR() do { asm volatile("s_waitcnt lgkmcnt(0)" ::: "memory"); __builtin_amdgcn_s_barrier(); asm volatile("" ::: "memory"); } while (0)

constexpr int S = 2048, D = 640, F = 1536, FH = 768;
constexpr float EPS = 1e-5f;
constexpr unsigned O_CNT = 0;
constexpr unsigned CNT_B = 0, CNT_C = 1024, CNT_D = 2048, CNT_A = 3072, CNT_N = 4096;
constexpr unsigned O_STZ = O_CNT + CNT_N * 128;
constexpr unsigned SZ_ST = 32 * 8 * 64 * 8;
constexpr unsigned O_STX = O_STZ + 2 * SZ_ST;
constexpr unsigned O_CS = O_STX + 4 * SZ_ST;
constexpr unsigned O_KVT = O_CS + 8192;
constexpr unsigned SZ_X = S * D * 2;
constexpr unsigned O_KS1 = O_KVT + SZ_X;
constexpr unsigned O_YF = O_KS1 + SZ_X;
constexpr unsigned O_XF = O_YF + SZ_X;
constexpr unsigned O_W1F = O_XF + 2 * SZ_X;
constexpr unsigned O_W3F = O_W1F + D * F * 2;
constexpr unsigned SZ_Z = S * FH * 2;
constexpr unsigned O_Z2F = O_W3F + FH * D * 2;
constexpr unsigned O_UF = O_Z2F + 2 * SZ_Z;
constexpr unsigned WS_END = O_UF + 2 * SZ_Z;

DI float bf2f(u16 b) { return __uint_as_float(((unsigned)b) << 16); }
DI float lo16(unsigned u) { return __uint_as_float(u << 16); }
DI float hi16(unsigned u) { return __uint_as_float(u & 0xffff0000u); }
DI unsigned pk2(f32x2 v) { bf2v r = __builtin_convertvector(v, bf2v); return __builtin_bit_cast(unsigned, r); }
DI unsigned pk(float a, float b) { return pk2(f32x2{a, b}); }
DI float rbf(float a) { return lo16(pk(a, a)); }
DI f32x2 up2(unsigned u) { return f32x2{lo16(u), hi16(u)}; }
DI f32x2 rbf2(f32x2 v) { return up2(pk2(v)); }
DI void mf_acc(f32x4& d, v4u a, v4u b) { asm volatile("v_mfma_f32_16x16x32_bf16 %0, %1, %2, %0" : "+v"(d) : "v"(a), "v"(b)); }
DI void mf_ini(f32x4& d, v4u a, v4u b) { asm volatile("v_mfma_f32_16x16x32_bf16 %0, %1, %2, 0" : "=&v"(d) : "v"(a), "v"(b)); }
DI void mf_acc_a(f32x4& d, v4u a, v4u b) { asm volatile("v_mfma_f32_16x16x32_bf16 %0, %1, %2, %0" : "+v"(d) : "v"(a), "a"(b)); }
DI void mf_ini_a(f32x4& d, v4u a, v4u b) { asm volatile("v_mfma_f32_16x16x32_bf16 %0, %1, %2, 0" : "=&v"(d) : "v"(a), "a"(b)); }
DI void mf_inic_a(f32x4& d, v4u a, v4u b, f32x4 c) { asm volatile("v_mfma_f32_16x16x32_bf16 %0, %1, %2, %3" : "=&v"(d) : "v"(a), "a"(b), "v"(c)); }
DI void mf16_ini(f32x4& d, v2u a, v2u b) { asm volatile("s_nop 1\n\tv_mfma_f32_16x16x16_bf16 %0, %1, %2, 0" : "=&v"(d) : "v"(a), "v"(b)); }
#define MFENCE2(a, b) asm volatile("s_nop 7" : "+v"(a), "+v"(b))
#define MFENCE4(a, b, c, d) asm volatile("s_nop 7" : "+v"(a), "+v"(b), "+v"(c), "+v"(d))
#define MFENCE6(a, b, c, d, e, f) asm volatile("s_nop 7" : "+v"(a), "+v"(b), "+v"(c), "+v"(d), "+v"(e), "+v"(f))
#define MFENCE8(a, b, c, d, e, f, g, h) asm volatile("s_nop 7" : "+v"(a), "+v"(b), "+v"(c), "+v"(d), "+v"(e), "+v"(f), "+v"(g), "+v"(h))
DI int piperm(int tile, int c) { return 32 * (tile >> 1) + 8 * (c >> 2) + 4 * (tile & 1) + (c & 3); }
typedef __amdgpu_buffer_rsrc_t rsrc_t;
DI rsrc_t mkrs(const void* p, unsigned bytes) { return __builtin_amdgcn_make_buffer_rsrc((void*)p, 0, (int)bytes, 0x00020000); }
constexpr int SC1 = 16;
template <int AUX> DI v4u ld128s(rsrc_t rs, unsigned voff, unsigned soff) { return __builtin_amdgcn_raw_buffer_load_b128(rs, voff, soff, AUX); }
template <int AUX> DI v2u ld64s(rsrc_t rs, unsigned voff, unsigned soff) { return __builtin_amdgcn_raw_buffer_load_b64(rs, voff, soff, AUX); }
template <int AUX> DI void st128s(v4u v, rsrc_t rs, unsigned voff, unsigned soff) { __builtin_amdgcn_raw_buffer_store_b128(v, rs, voff, soff, AUX); }
template <int AUX> DI void st64s(v2u v, rsrc_t rs, unsigned voff, unsigned soff) { __builtin_amdgcn_raw_buffer_store_b64(v, rs, voff, soff, AUX); }
DI v2u trrd(LAS unsigned char* a) { return __builtin_bit_cast(v2u, __builtin_amdgcn_ds_read_tr16_b64_v4i16((LAS s16x4*)a)); }

struct Params {
    const u16 *q, *k, *v, *shk, *ssmk, *ssmD, *w_in, *w_sp, *b_sp, *w_out;
    u16* out;
    unsigned char* ws;
};

constexpr int PRE_SH = 160, PRE_KV = 320, PRE_W1 = 120, PRE_W3 = 48, PRE_CS = 24, PRE_Z = 24;
constexpr int PRE_N = PRE_SH + PRE_KV + PRE_W1 + PRE_W3 + PRE_CS + PRE_Z;
DI void pre_body(const Params& p, LAS unsigned char* M) {
    int b = blockIdx.x;
    const int tid = threadIdx.x;
    unsigned char* ws = p.ws;
    if (b < PRE_KV) {
        const int cg = b % 10, rt = b / 10;
        const int ch0 = cg * 64 + (tid & 7) * 8, rl = (tid >> 3) * 2, s0 = rt * 64 + rl;
        v4u kk[5], vv[2];
#pragma unroll
        for (int i = 0; i < 5; ++i) {
            int s = s0 - 3 + i;
            kk[i] = s >= 0 ? *(const v4u*)(p.k + (size_t)s * D + ch0) : v4u{0, 0, 0, 0};
        }
#pragma unroll
        for (int i = 0; i < 2; ++i) vv[i] = *(const v4u*)(p.v + (size_t)(s0 + i) * D + ch0);
        v4u sh[4];
#pragma unroll
        for (int c = 0; c < 4; ++c) sh[c] = *(const v4u*)(p.shk + (size_t)(ch0 + 2 * c) * 4);
        const int pos = (rl & ~7) + 6 - (rl & 6);
#pragma unroll
        for (int c2 = 0; c2 < 4; ++c2) {
            f32x2 w0 = {lo16(sh[c2].x), lo16(sh[c2].z)}, w1 = {hi16(sh[c2].x), hi16(sh[c2].z)}, w2 = {lo16(sh[c2].y), lo16(sh[c2].w)}, w3 = {hi16(sh[c2].y), hi16(sh[c2].w)};
            f32x2 kf[5];
#pragma unroll
            for (int i = 0; i < 5; ++i) kf[i] = up2(kk[i][c2]);
            f32x2 r[2];
#pragma unroll
            for (int i = 0; i < 2; ++i) {
                f32x2 a = rbf2(kf[i] * w3);
                a = rbf2(a + rbf2(kf[i + 1] * w2));
                a = rbf2(a + rbf2(kf[i + 2] * w1));
                a = rbf2(a + rbf2(kf[i + 3] * w0));
                r[i] = a * up2(vv[i][c2]);
            }
            *(LAS unsigned*)(M + ((tid & 7) * 8 + 2 * c2) * 144 + pos * 2) = pk(r[1].x, r[0].x);
            *(LAS unsigned*)(M + ((tid & 7) * 8 + 2 * c2 + 1) * 144 + pos * 2) = pk(r[1].y, r[0].y);
        }
        __syncthreads();
#pragma unroll
        for (int i = 0; i < 2; ++i) {
            int id = i * 256 + tid, ch = id >> 3, part = id & 7;
            v4u o = *(LAS v4u*)(M + ch * 144 + part * 16);
            *(v4u*)(ws + O_KVT + (size_t)(cg * 64 + ch) * (S * 2) + rt * 128 + part * 16) = o;
        }
        return;
    }
    b -= PRE_KV;
    if (b < PRE_SH) {
        const rsrc_t rsk = mkrs(p.ssmk, D * S * 2);
        v4u a[4]; unsigned n[4];
#pragma unroll
        for (int i = 0; i < 4; ++i) {
            a[i] = ld128s<0>(rsk, tid * 16, b * 16384 + i * 4096);
            n[i] = __builtin_amdgcn_raw_buffer_load_b32(rsk, tid * 16 + 16, b * 16384 + i * 4096, 0);
        }
#pragma unroll
        for (int i = 0; i < 4; ++i) {
            v4u o = {__builtin_amdgcn_alignbit(a[i].y, a[i].x, 16), __builtin_amdgcn_alignbit(a[i].z, a[i].y, 16), __builtin_amdgcn_alignbit(a[i].w, a[i].z, 16), __builtin_amdgcn_alignbit(n[i], a[i].w, 16)};
            *(v4u*)(ws + O_KS1 + b * 16384 + i * 4096 + tid * 16) = o;
        }
        return;
    }
    b -= PRE_SH;
    const int l = tid & 63, w = tid >> 6, q = l >> 4, qq = (l & 15) >> 2, pp = l & 3;
    if (b < PRE_W1) {
        const int kk = b / 6, fc = b % 6;
        v4u x[4];
#pragma unroll
        for (int i = 0; i < 4; ++i) { int id = i * 256 + tid; x[i] = *(const v4u*)(p.w_in + (size_t)(32 * kk + (id >> 5)) * F + fc * 256 + (id & 31) * 8); }
#pragma unroll
        for (int i = 0; i < 4; ++i) { int id = i * 256 + tid; *(LAS v4u*)(M + (id >> 5) * 528 + (id & 31) * 16) = x[i]; }
        __syncthreads();
#pragma unroll
        for (int r = 0; r < 4; ++r) {
            int t = w * 4 + r;
            LAS unsigned char* a = M + (8 * q + qq) * 528 + (32 * (t >> 1) + 8 * pp + 4 * (t & 1)) * 2;
            v2u lo = trrd(a), hi = trrd(a + 4 * 528);
            *(v4u*)(ws + O_W1F + (((fc * 16 + t) * 20 + kk) * 64 + l) * 16) = v4u{lo.x, lo.y, hi.x, hi.y};
        }
        return;
    }
    b -= PRE_W1;
    if (b < PRE_W3) {
        const int kk = b >> 1, dc = b & 1;
        v4u x[5];
#pragma unroll
        for (int i = 0; i < 5; ++i) { int id = i * 256 + tid; x[i] = *(const v4u*)(p.w_out + (size_t)(32 * kk + id / 40) * D + dc * 320 + (id % 40) * 8); }
#pragma unroll
        for (int i = 0; i < 5; ++i) { int id = i * 256 + tid; *(LAS v4u*)(M + (id / 40) * 656 + (id % 40) * 16) = x[i]; }
        __syncthreads();
#pragma unroll
        for (int t5 = 0; t5 < 5; ++t5) {
            int dl = w * 80 + (t5 < 4 ? 32 * (t5 >> 1) + 8 * pp + 4 * (t5 & 1) : 64 + 4 * pp);
            LAS unsigned char* a = M + (8 * q + qq) * 656 + dl * 2;
            v2u lo = trrd(a), hi = trrd(a + 4 * 656);
            *(v4u*)(ws + O_W3F + ((((dc * 4 + w) * 5 + t5) * 24 + kk) * 64 + l) * 16) = v4u{lo.x, lo.y, hi.x, hi.y};
        }
        return;
    }
    b -= PRE_W3;
    if (b < PRE_CS) {
        const int c8 = tid & 7, rg = tid >> 3;
        f32x2 a[4] = {};
        v4u x[20];
#pragma unroll
        for (int i = 0; i < 20; ++i) x[i] = *(const v4u*)(p.w_in + (size_t)(rg * 20 + i) * F + b * 64 + c8 * 8);
#pragma unroll
        for (int i = 0; i < 20; ++i)
#pragma unroll
            for (int e = 0; e < 4; ++e) a[e] += up2(x[i][e]);
        LAS float* sf = (LAS float*)M;
#pragma unroll
        for (int e = 0; e < 4; ++e) { sf[rg * 65 + c8 * 8 + 2 * e] = a[e].x; sf[rg * 65 + c8 * 8 + 2 * e + 1] = a[e].y; }
        __syncthreads();
        if (tid < 64) {
            float s = 0.f;
#pragma unroll
            for (int r = 0; r < 32; ++r) s += sf[r * 65 + tid];
            ((float*)(ws + O_CS))[b * 64 + tid] = s;
        }
        return;
    }
    b -= PRE_CS;
    {
        for (int i = b * 256 + tid; i < (int)CNT_N; i += PRE_Z * 256) *(unsigned*)(ws + O_CNT + i * 128) = 0u;
        for (int i = b * 256 + tid; i < (int)(6 * SZ_ST / 16); i += PRE_Z * 256) *(v4u*)(ws + O_STZ + i * 16) = v4u{~0u, ~0u, ~0u, ~0u};
    }
}

__global__ void __launch_bounds__(256) k_pre(Params p) {
    __shared__ __attribute__((aligned(16))) unsigned char sm[32 * 656];
    pre_body(p, (LAS unsigned char*)sm);
}

constexpr int L_XA = 0;
constexpr int L_W3 = 0;
constexpr int L_Z1 = 122880;
constexpr int L_RED = L_Z1 + 12288;
constexpr int L_SX = L_RED + 24576;
constexpr int L_ZS = L_SX + 512;
constexpr int L_ZN = L_ZS + 2048;
constexpr int L_CS = L_ZN + 512;
constexpr int L_PROF = L_CS + 768;
constexpr int L_END = L_PROF + 256;
static_assert(L_END <= 163840, "lds");
constexpr int CV_CPX = 4416, CV_CPY = 2368;
constexpr int CV_CHY = 4 * CV_CPY + 32 * 160 + 512;
constexpr int CV_WREG = 2 * CV_CHY;
constexpr int CV_OUT = 4 * CV_WREG;
static_assert(CV_OUT + 8 * 2176 <= L_SX && CV_CHY % 256 == 0, "conv lds");
constexpr unsigned SPIN_CAP = 1u << 18;
#define STAMP(i)

DI void wait_cnt(gu32* c, unsigned want) {
    for (unsigned sp = 0; sp < SPIN_CAP; ++sp) {
        if (__hip_atomic_load(c, RLX_AGENT) >= want) break;
    }
    __builtin_amdgcn_fence(__ATOMIC_ACQUIRE, "wavefront");
    CBAR();
}
DI float rsum4(float v) {
    unsigned u = __float_as_uint(v);
    auto a = __builtin_amdgcn_permlane16_swap(u, u, false, false);
    v = __uint_as_float(a[0]) + __uint_as_float(a[1]);
    u = __float_as_uint(v);
    auto b = __builtin_amdgcn_permlane32_swap(u, u, false, false);
    return __uint_as_float(b[0]) + __uint_as_float(b[1]);
}
DI float tred4(float a, float b, float c, float d) {
    auto p = __builtin_amdgcn_permlane16_swap(__float_as_uint(a), __float_as_uint(b), false, false);
    float ab = __uint_as_float(p[0]) + __uint_as_float(p[1]);
    auto q = __builtin_amdgcn_permlane16_swap(__float_as_uint(c), __float_as_uint(d), false, false);
    float cd = __uint_as_float(q[0]) + __uint_as_float(q[1]);
    auto r = __builtin_amdgcn_permlane32_swap(__float_as_uint(ab), __float_as_uint(cd), false, false);
    return __uint_as_float(r[0]) + __uint_as_float(r[1]);
}
DI f32x2 gelu2(f32x2 x) {
    constexpr float C1 = -2.f * 0.7978845608f * 0.044715f * 1.4426950409f, C0 = -2.f * 0.7978845608f * 1.4426950409f;
    f32x2 t = x * (x * x * C1 + C0);
    f32x2 e = {__builtin_amdgcn_exp2f(t.x), __builtin_amdgcn_exp2f(t.y)};
    e = e + 1.f;
    f32x2 r = {__builtin_amdgcn_rcpf(e.x), __builtin_amdgcn_rcpf(e.y)};
    return x * r;
}
DI f32x2 fin_stats(float mean, float var) {
    float sd = rbf(__builtin_amdgcn_sqrtf(rbf(rbf(var) + EPS)));
    return f32x2{rbf(mean), __builtin_amdgcn_rcpf(sd)};
}
template <int N> DI f32x2 merge_stats(v2u ua, v2u ub) {
    f32x2 a = __builtin_bit_cast(f32x2, ua), b = __builtin_bit_cast(f32x2, ub);
    float mean = rsum4(a.x + b.x) * (1.f / (8 * N));
    float da = a.x * (1.f / N) - mean, db = b.x * (1.f / N) - mean;
    float m2 = rsum4(a.y + b.y + (float)N * (da * da + db * db));
    return fin_stats(mean, m2 * (1.f / (8 * N)));
}
DI v2u canon(float a, float b) {
    v2u o = {__float_as_uint(a), __float_as_uint(b)};
    if (o.x == ~0u) o.x = 0x7fc00000u;
    if (o.y == ~0u) o.y = 0x7fc00000u;
    return o;
}
DI f32x2 slice_stats(const unsigned (&o)[10]) {
    f32x2 v[10];
#pragma unroll
    for (int e = 0; e < 10; ++e) v[e] = up2(o[e]);
    f32x2 s = ((v[0] + v[1]) + (v[2] + v[3])) + ((v[4] + v[5]) + (v[6] + v[7])) + (v[8] + v[9]);
    float sum = rsum4(s.x + s.y);
    float mu = sum * (1.f / 80.f);
    f32x2 q = {0.f, 0.f};
#pragma unroll
    for (int e = 0; e < 10; ++e) { f32x2 d = v[e] - mu; q += d * d; }
    return f32x2{sum, rsum4(q.x + q.y)};
}
DI void poll2(rsrc_t rs, unsigned voff, unsigned so, v2u& sa, v2u& sb) {
    for (unsigned sp = 0; sp < SPIN_CAP; ++sp) {
        CBAR();
        sa = ld64s<SC1>(rs, voff, so); sb = ld64s<SC1>(rs, voff, so + 512);
        bool ok = sa.x != ~0u && sa.y != ~0u && sb.x != ~0u && sb.y != ~0u;
        if (__all(ok)) break;
    }
}
DI void glds16(rsrc_t rs, unsigned lds_addr, unsigned voff, unsigned soff) {
    asm volatile("s_mov_b32 m0, %0\n\ts_nop 0\n\tbuffer_load_dwordx4 %1, %2, %3 offen lds" :: "s"(lds_addr), "v"(voff), "s"(rs), "s"(soff) : "memory");
}
constexpr int NPRE1 = 10, NPRE = 20;
constexpr int W1WIN = 20;
constexpr int ZWIN = 12;
constexpr int UWIN = 4;
constexpr int Z1SPLIT = 10;

__global__ void __launch_bounds__(256, 1) k_main(Params p) {
    extern __shared__ __attribute__((aligned(16))) unsigned char smem[];
    LAS unsigned char* L = (LAS unsigned char*)smem;
    const int tid = threadIdx.x, w = __builtin_amdgcn_readfirstlane(tid >> 6);
    int l = tid & 63;
    int c = l & 15, g = l >> 4;
    const int bx = blockIdx.x & 7, bi = blockIdx.x >> 3;
#define MAP 2
    const int rb = (bx >> MAP) * (4 << MAP) + (bi >> (3 - MAP)), j = (bx & ((1 << MAP) - 1)) * (8 >> MAP) + (bi & ((8 >> MAP) - 1));
    const int wg = rb * 8 + j;
    const rsrc_t rs = mkrs(p.ws, WS_END);
    gu32* cnt = (gu32*)(p.ws + O_CNT);
    unsigned l16 = l * 16, l8 = l * 8;
#define RELANE() do { asm volatile("" : "+v"(l)); c = l & 15; g = l >> 4; l16 = l * 16; l8 = l * 8; } while (0)
    const int wo = w & 1;
    const int ftb = 4 + (w >> 1);
    const int rot = rb % 3;
    const unsigned ldsbase = (unsigned)(size_t)smem;
    const unsigned w1a_ = O_W1F + (6 * j + w) * 20 * 1024, w1b_ = O_W1F + (6 * j + ftb) * 20 * 1024;
#define W1BASES() unsigned w1a = w1a_, w1b = w1b_; OPQ(w1a); OPQ(w1b)
#define w1off(idx) ((((idx) & 1) ? w1b : w1a) + (((idx) < 40 ? 48 * 20 : 0) + (((idx) % 40) >> 1)) * 1024)

    int layer = 0;
    STAMP(0);
    unsigned xs[10], ys[10];
    v4u Bw[4][16];
    {
        const bool real = blockIdx.x < 240;
        const int cu = real ? blockIdx.x : blockIdx.x - 240;
        const bool isX = cu < 160;
        const int chA = isX ? cu * 4 + w : (cu - 160) * 8 + 2 * w;
        const int CPY = isX ? CV_CPX : CV_CPY;
        const int chB = __builtin_amdgcn_readfirstlane(isX ? chA : chA + 1);
        LAS unsigned char* RG = L + w * CV_WREG;
        const rsrc_t rsk = mkrs(p.ssmk, D * S * 2);
        const int seg1 = isX ? 0 : CV_CHY;
        float Dv0 = bf2f(p.ssmD[chA]), Dv1 = bf2f(p.ssmD[chB]);
        v2u k01s[2];
        k01s[0] = *(const v2u*)(p.ssmk + (size_t)chA * S);
        k01s[1] = *(const v2u*)(p.ssmk + (size_t)chB * S);
        SB();
        const unsigned rgl = ldsbase + w * CV_WREG;
#pragma unroll
        for (int i = 0; i < 6; ++i) {
            const int sl = (isX ? i : i % 3) * 64 + l;
            const int blk = (sl * 6554) >> 16, chk = sl - blk * 10;
            const unsigned vo = blk * 128 + (chk > 7 ? 7 : chk) * 16;
            const unsigned dst = rgl + 4 * CPY + 2560 + (isX ? i * 1024 : (i / 3) * CV_CHY + (i % 3) * 1024);
            glds16(rs, dst, vo, O_KVT + (i < 3 ? chA : chB) * 4096);
        }
#pragma unroll
        for (int r = 0; r < 4; ++r) {
            const unsigned so = (r < 2 ? chA : chB) * 4096 + (isX ? r : (r & 1)) * 1024;
            const unsigned dst = rgl + (isX ? r * 1024 : (r >> 1) * CV_CHY + (r & 1) * 1024) + 128;
#pragma unroll
            for (int cp = 0; cp < 4; ++cp) {
                if (cp & 1) glds16(rs, dst + cp * CPY, l16, O_KS1 + so + 2 * (cp - 1));
                else glds16(rsk, dst + cp * CPY, l16, so + 2 * cp);
            }
        }
#pragma unroll
        for (int s_ = 0; s_ < 2; ++s_) {
            LAS unsigned char* rk = RG + s_ * seg1;
            LAS unsigned char* rv = rk + 4 * CPY;
            *(LAS v4u*)(rv + l16) = v4u{0, 0, 0, 0};
            *(LAS v4u*)(rv + 1024 + l16) = v4u{0, 0, 0, 0};
            const v2u k01 = k01s[s_];
            const int cpy = (l >> 3) & 3, U = l & 7;
            v4u o = {0, 0, 0, 0};
            if (U == 7 && l < 32) {
                if (cpy == 1) o.w = k01.x << 16;
                if (cpy == 2) o.w = k01.x;
                if (cpy == 3) { o.z = k01.x << 16; o.w = __builtin_amdgcn_alignbit(k01.y, k01.x, 16); }
            }
            *(LAS v4u*)(l < 32 ? rk + cpy * CPY + U * 16 : rv + 2048 + (l - 32) * 16) = o;
        }
        float csv;
        {
            const int cc0 = 10 * j + g, cc1 = cc0 + 4, cc2 = 10 * j + 8 + (g >> 1);
            const u16* xrow = p.q + (size_t)(rb * 64 + piperm(w, c)) * D;
            v4u a = *(const v4u*)(xrow + cc0 * 8), b = *(const v4u*)(xrow + cc1 * 8);
            v2u d = *(const v2u*)(xrow + cc2 * 8 + (g & 1) * 4);
            xs[0] = a.x; xs[1] = a.y; xs[2] = a.z; xs[3] = a.w; xs[4] = b.x; xs[5] = b.y; xs[6] = b.z; xs[7] = b.w; xs[8] = d.x; xs[9] = d.y;
            csv = ((const float*)(p.ws + O_CS))[(tid < 96 ? 0 : FH - 96) + 96 * j + (tid < 192 ? tid : 0)];
        }
        SB();
        WAITV(4);
        STAMP(17);
        LAS unsigned char* ga0 = RG + ((c + 1) & 3) * CPY + (((c + 25) >> 2) - 2 * g) * 8;
        LAS unsigned char* ga1 = ga0 + (isX ? 64 * 32 : CV_CHY);
        LAS unsigned char* bp0 = RG + 4 * CPY + (1 + (isX ? 16 : 0) + c) * 160 + g * 16;
        LAS unsigned char* bp1 = isX ? bp0 - 16 * 160 : bp0 + CV_CHY;
        const rsrc_t rsp = mkrs(p.w_sp, S * S * 2);
        unsigned spb = (unsigned)(rb * 64) * (S * 2) + 1024 * w; OPQ(spb);
        v4u raw[64];
        auto ldG = [&](LAS unsigned char* ga, int n) -> v4u {
            v2u lo = *(volatile LAS v2u*)(ga + (n + 2) * 32), hi = *(volatile LAS v2u*)(ga + (n + 2) * 32 + 8);
            return v4u{lo.x, lo.y, hi.x, hi.y};
        };
        f32x4 acc[4];
        v4u gg[2][6], bb[2][2];
        gg[0][0] = ldG(ga0, -2); gg[0][1] = ldG(ga0, -1);
        bb[0][1] = *(LAS v4u*)(bp0 + 15 * 160 + 64);
        gg[0][2] = ldG(ga0, 0); gg[0][3] = ldG(ga0, 1);
        bb[0][0] = *(LAS v4u*)(bp0 + 15 * 160);
        gg[0][4] = ldG(ga0, 2); gg[0][5] = ldG(ga0, 3);
        SB();
        auto conv_epi = [&](int s) {
            MFENCE4(acc[0], acc[1], acc[2], acc[3]);
            const float Dv = s ? Dv1 : Dv0;
            const int chl = isX ? w : 2 * w + s;
            LAS unsigned char* kvb = RG + (s ? seg1 : 0) + 4 * CPY + (16 + (isX ? 16 : 0) + c) * 160;
            LAS unsigned char* ob = L + CV_OUT + chl * 2176 + c * 136 + (g & 1) * 32 + (g >> 1) * 8;
#pragma unroll
            for (int rho = 0; rho < 4; ++rho) {
                v2u kvv = *(LAS v2u*)(kvb + (16 * rho + 8 * (g >> 1) + 4 - 4 * (g & 1)) * 2);
                f32x2 k01 = {hi16(kvv.y), lo16(kvv.y)}, k23 = {hi16(kvv.x), lo16(kvv.x)};
                unsigned y01 = pk2(rbf2(f32x2{acc[rho][0], acc[rho][1]}) + rbf2(k01 * Dv));
                unsigned y23 = pk2(rbf2(f32x2{acc[rho][2], acc[rho][3]}) + rbf2(k23 * Dv));
                *(LAS v2u*)(ob + (rho >> 1) * 64 + (rho & 1) * 16) = v2u{y01, y23};
            }
        };
#pragma unroll
        for (int st = 0; st < 32; ++st) {
            const int P = st & 1, N = P ^ 1;
            const int ns = st + 1, nseg = ns >> 4, nmm = ns & 15;
            LAS unsigned char* ga = nseg ? ga1 : ga0;
            LAS unsigned char* bp = nseg ? bp1 : bp0;
            const bool more = ns < 32;
            if (st == 0) { mf_ini(acc[0], gg[P][0], bb[P][1]); } else mf_acc(acc[0], gg[P][0], bb[P][1]);
            if (more) bb[N][1] = *(LAS v4u*)(bp + (15 - nmm) * 160 + 64);
            SB();
            if (st == 0) { mf_ini(acc[1], gg[P][1], bb[P][1]); } else mf_acc(acc[1], gg[P][1], bb[P][1]);
            if (more && nmm == 0) { gg[N][0] = ldG(ga, -2); gg[N][1] = ldG(ga, -1); }
            if (more) gg[N][2] = ldG(ga, 4 * nmm);
            SB();
            if (st == 0) { mf_ini(acc[2], gg[P][2], bb[P][1]); } else mf_acc(acc[2], gg[P][2], bb[P][1]);
            if (more) gg[N][3] = ldG(ga, 4 * nmm + 1);
            SB();
            if (st == 0) { mf_ini(acc[3], gg[P][3], bb[P][1]); } else mf_acc(acc[3], gg[P][3], bb[P][1]);
            if (more) bb[N][0] = *(LAS v4u*)(bp + (15 - nmm) * 160);
            SB();
            mf_acc(acc[0], gg[P][2], bb[P][0]);
            if (more) gg[N][4] = ldG(ga, 4 * nmm + 2);
            SB();
            mf_acc(acc[1], gg[P][3], bb[P][0]);
            if (more) gg[N][5] = ldG(ga, 4 * nmm + 3);
            SB();
            mf_acc(acc[2], gg[P][4], bb[P][0]);
            {
                const int i0 = 2 * st, t = i0 >> 4, r = i0 & 15, tt = (w + t) & 3;
                raw[i0] = ld128s<0>(rsp, l16, spb + (unsigned)(32 * (tt >> 1) + 4 * (tt & 1) + 8 * (r >> 2) + (r & 3)) * (S * 2));
            }
            SB();
            mf_acc(acc[3], gg[P][5], bb[P][0]);
            {
                const int i0 = 2 * st + 1, t = i0 >> 4, r = i0 & 15, tt = (w + t) & 3;
                raw[i0] = ld128s<0>(rsp, l16, spb + (unsigned)(32 * (tt >> 1) + 4 * (tt & 1) + 8 * (r >> 2) + (r & 3)) * (S * 2));
            }
            if (more && nmm != 0) { gg[N][0] = gg[P][4]; gg[N][1] = gg[P][5]; }
            SB();
            if (st == 15 && !isX) {
                conv_epi(0);
#pragma unroll
                for (int r = 0; r < 4; ++r) acc[r] = f32x4{0, 0, 0, 0};
                asm volatile("s_nop 3" : "+v"(acc[0]), "+v"(acc[1]), "+v"(acc[2]), "+v"(acc[3]));
            }
        }
        conv_epi(1);
        {
            f32x2 ps = slice_stats(xs);
            if (g == 0) st64s<SC1>(canon(ps.x, ps.y), rs, c * 8, O_STX + (rb * 8 * 64 + w * 16) * 8 + j * 512);
            if (tid < 192) *(LAS float*)(L + L_CS + tid * 4) = csv;
        }
        BAR();
        STAMP(18);
        if (real) {
            const int ch0 = isX ? cu * 4 : (cu - 160) * 8;
            const unsigned yb = O_YF + (isX ? 64 * 20480 : 0) + (ch0 >> 5) * 1024 + ((ch0 >> 3) & 3) * 128 + ((ch0 >> 2) & 1) * 512;
            const unsigned vof = (tid >> 2) * 20480 + (tid & 3) * 32;
            LAS unsigned char* ib = L + CV_OUT + (tid >> 4) * 136 + (tid & 15) * 8;
#pragma unroll
            for (int gI = 0; gI < 2; ++gI) {
                if (gI == 0 || !isX) {
                    v2u c0 = *(LAS v2u*)(ib + (4 * gI) * 2176), c1 = *(LAS v2u*)(ib + (4 * gI + 1) * 2176), c2 = *(LAS v2u*)(ib + (4 * gI + 2) * 2176), c3 = *(LAS v2u*)(ib + (4 * gI + 3) * 2176);
                    v4u o0 = {__builtin_amdgcn_perm(c1.x, c0.x, 0x05040100), __builtin_amdgcn_perm(c3.x, c2.x, 0x05040100), __builtin_amdgcn_perm(c1.x, c0.x, 0x07060302), __builtin_amdgcn_perm(c3.x, c2.x, 0x07060302)};
                    v4u o1 = {__builtin_amdgcn_perm(c1.y, c0.y, 0x05040100), __builtin_amdgcn_perm(c3.y, c2.y, 0x05040100), __builtin_amdgcn_perm(c1.y, c0.y, 0x07060302), __builtin_amdgcn_perm(c3.y, c2.y, 0x07060302)};
                    st128s<SC1>(o0, rs, vof, yb + gI * 512); st128s<SC1>(o1, rs, vof, yb + gI * 512 + 16);
                }
            }
            DRAIN();
            BAR();
            if (tid < 16) __hip_atomic_fetch_add(cnt + (CNT_A + ((isX ? 16 : 0) + tid) * 8 + ch0 / 80) * 32, 1u, RLX_AGENT);
        } else {
            BAR();
        }
        STAMP(19);
        {
            LAS unsigned char* T = L + w * 16384;
            const unsigned tb = ldsbase + w * 16384;
#pragma unroll
            for (int t = 0; t < 4; ++t) {
#pragma unroll
                for (int r = 0; r < 16; ++r) {
                    const unsigned a = tb + ((l ^ r) << 4);
                    asm volatile("ds_write_b128 %0, %1 offset:%2" :: "v"(a), "a"(raw[16 * t + r]), "i"(r * 1024) : "memory");
                }
#pragma unroll
                for (int kk = 0; kk < 16; ++kk) Bw[t][kk] = *(LAS v4u*)(T + c * 1024 + (((4 * kk + g) ^ c) << 4));
                asm volatile("s_waitcnt lgkmcnt(0)" ::: "memory");
                SB();
            }
        }
        BAR();
        wait_cnt(cnt + (CNT_A + wg) * 32, rb >= 16 ? 20u : 10u);
        {
            const int cc0 = 10 * j + g, cc1 = cc0 + 4, cc2 = 10 * j + 8 + (g >> 1);
            const unsigned yo = O_YF + (rb * 4 + w) * 20 * 1024;
            v2u y0 = ld64s<SC1>(rs, (cc0 >> 2) * 1024 + ((cc0 & 3) * 16 + c) * 8, yo), y1 = ld64s<SC1>(rs, (cc0 >> 2) * 1024 + ((cc0 & 3) * 16 + c) * 8, yo + 512);
            v2u y2 = ld64s<SC1>(rs, (cc1 >> 2) * 1024 + ((cc1 & 3) * 16 + c) * 8, yo), y3 = ld64s<SC1>(rs, (cc1 >> 2) * 1024 + ((cc1 & 3) * 16 + c) * 8, yo + 512);
            v2u y4 = ld64s<SC1>(rs, (cc2 >> 2) * 1024 + ((cc2 & 3) * 16 + c) * 8 + (g & 1) * 512, yo);
            ys[0] = y0.x; ys[1] = y0.y; ys[2] = y1.x; ys[3] = y1.y; ys[4] = y2.x; ys[5] = y2.y; ys[6] = y3.x; ys[7] = y3.y; ys[8] = y4.x; ys[9] = y4.y;
        }
    }
    const float bsp = bf2f(p.b_sp[rb * 64 + piperm(w, c)]);
    v2u ident;
    {
        int e = c - 4 * g;
        ident.x = (e == 0 ? 0x3f80u : 0u) | (e == 1 ? 0x3f800000u : 0u);
        ident.y = (e == 2 ? 0x3f80u : 0u) | (e == 3 ? 0x3f800000u : 0u);
    }

    for (layer = 0; layer < 2; ++layer) {
        RELANE();
        v4u w1[80];
        W1BASES();
        {
            const unsigned so = O_STX + (layer * 2) * SZ_ST + (rb * 8 * 64 + w * 16) * 8;
            STAMP(1);
            v2u sa, sb;
            poll2(rs, g * 1024 + c * 8, so, sa, sb);
            STAMP(2);
            f32x2 st = merge_stats<80>(sa, sb);
#pragma unroll
            for (int e = 0; e < 10; ++e) {
                f32x2 x = up2(xs[e]);
                f32x2 q = (x - st.x) * st.y;
                xs[e] = pk2(q * up2(ys[e]) + x);
            }
            const int cc0 = 10 * j + g, cc1 = cc0 + 4, cc2 = 10 * j + 8 + (g >> 1);
            const unsigned xo = O_XF + layer * SZ_X + (rb * 4 + w) * 20 * 1024;
            st128s<SC1>(v4u{xs[0], xs[1], xs[2], xs[3]}, rs, c * 16 + cc0 * 256, xo);
            st128s<SC1>(v4u{xs[4], xs[5], xs[6], xs[7]}, rs, c * 16 + cc1 * 256, xo);
            st64s<SC1>(v2u{xs[8], xs[9]}, rs, c * 16 + cc2 * 256 + (g & 1) * 8, xo);
            f32x2 ps = slice_stats(xs);
            if (g == 0) st64s<SC1>(v2u{__float_as_uint(ps.x), __float_as_uint(ps.y)}, rs, c * 8, so + SZ_ST + j * 512);
            SB();
#pragma unroll
            for (int i = 0; i < NPRE1; ++i) w1[i] = ld128s<0>(rs, l16, w1off(i));
            SB();
            WAITV(NPRE1);
            if (l < 8) __hip_atomic_fetch_add(cnt + (CNT_D + (rb * 8 + l) * 4 + w) * 32, 1u, RLX_AGENT);
            SB();
#pragma unroll
            for (int i = NPRE1; i < NPRE; ++i) w1[i] = ld128s<0>(rs, l16, w1off(i));
            SB();
            STAMP(3);
        }
        wait_cnt(cnt + (CNT_D + wg * 4 + w) * 32, 8u * (layer + 1));
        STAMP(4);
        v4u x[20];
        {
            const unsigned so = O_STX + (layer * 2 + 1) * SZ_ST + (rb * 8 * 64 + w * 16) * 8;
            unsigned xo = O_XF + layer * SZ_X + (rb * 4 + w) * 20 * 1024; OPQ(xo);
#pragma unroll
            for (int kk = 0; kk < 10; ++kk) x[kk] = ld128s<SC1>(rs, l16, xo + kk * 1024);
            v2u sa = ld64s<SC1>(rs, g * 1024 + c * 8, so), sb = ld64s<SC1>(rs, g * 1024 + c * 8, so + 512);
#pragma unroll
            for (int kk = 10; kk < 20; ++kk) x[kk] = ld128s<SC1>(rs, l16, xo + kk * 1024);
            SB();
#pragma unroll
            for (int kk = 0; kk < 10; ++kk) *(LAS v4u*)(L + L_XA + (w * 20 + kk) * 1024 + l16) = x[kk];
            f32x2 st = merge_stats<80>(sa, sb);
            if (g == 0) *(LAS f32x2*)(L + L_SX + (w * 16 + c) * 8) = f32x2{st.y, -st.x * st.y};
        }
        BAR();
        STAMP(5);
        f32x4 z2[6];
        f32x4 aa[6];
        LAS unsigned char* xa0 = L + L_XA + l16 + (2 * wo) * 20480;
        LAS unsigned char* xa1 = L + L_XA + l16 + (2 * (wo ^ 1)) * 20480;
#define XRD(kk, t) (*(LAS v4u*)(((t) < 2 ? xa0 : xa1) + ((t) & 1) * 20480 + (kk) * 1024))
        v4u xb[3][4];
        auto g1_steps = [&](int pass, int k0, int k1, int kstop) {
#pragma unroll
            for (int kk = k0; kk < k1; ++kk) {
                const int cur = kk % 3, nxt = (kk + 2) % 3;
                const int ia = pass * 40 + 2 * kk;
#pragma unroll
                for (int t = 0; t < 4; ++t) {
                    if (kk == 0) mf_ini(aa[t], w1[ia], xb[cur][t]); else mf_acc(aa[t], w1[ia], xb[cur][t]);
                    if (kk + 2 < kstop) xb[nxt][t] = XRD(kk + 2, t);
                    SB();
                }
#pragma unroll
                for (int t = 0; t < 2; ++t) {
                    if (kk == 0) mf_ini(aa[4 + t], w1[ia + 1], xb[cur][t]); else mf_acc(aa[4 + t], w1[ia + 1], xb[cur][t]);
                    if (ia + W1WIN < 80) w1[ia + W1WIN + t] = ld128s<0>(rs, l16, w1off(ia + W1WIN + t));
                    if (pass == 0 && kk >= 2 && kk < 7) *(LAS v4u*)(L + L_XA + (w * 20 + 10 + 2 * (kk - 2) + t) * 1024 + l16) = x[10 + 2 * (kk - 2) + t];
                    SB();
                }
            }
        };
        auto xb_fill = [&](int kk) {
#pragma unroll
            for (int t = 0; t < 4; ++t) xb[kk % 3][t] = XRD(kk, t);
#pragma unroll
            for (int t = 0; t < 4; ++t) xb[(kk + 1) % 3][t] = XRD(kk + 1, t);
            SB();
        };
        xb_fill(0);
        g1_steps(0, 0, 8, 10);
        BAR();
        g1_steps(0, 8, 20, 20);
        MFENCE6(aa[0], aa[1], aa[2], aa[3], aa[4], aa[5]);
        STAMP(6);
        {
            f32x4 csa = *(LAS f32x4*)(L + L_CS + (96 + piperm(w, 4 * g)) * 4), csb = *(LAS f32x4*)(L + L_CS + (96 + piperm(ftb, 4 * g)) * 4);
            f32x2 sx[4];
#pragma unroll
            for (int i = 0; i < 4; ++i) sx[i] = *(LAS f32x2*)(L + L_SX + ((i ^ (2 * wo)) * 16 + c) * 8);
            f32x2 s1[4], s2[4];
#pragma unroll
            for (int t = 0; t < 6; ++t) {
                f32x4 cs = t < 4 ? csa : csb;
                f32x4 v = aa[t] * sx[t & 3].x + cs * sx[t & 3].y;
                f32x2 a = gelu2(f32x2{v[0], v[1]}), b = gelu2(f32x2{v[2], v[3]});
                z2[t] = f32x4{a.x, a.y, b.x, b.y};
                if (t < 4) { s1[t] = a + b; s2[t] = a * a + b * b; }
                else { s1[t - 4] += a + b; s2[t - 4] += a * a; s2[t - 4] += b * b; }
            }
            float so_ = tred4(s1[0].x + s1[0].y, s1[1].x + s1[1].y, s1[2].x + s1[2].y, s1[3].x + s1[3].y);
            float qo_ = tred4(s2[0].x + s2[0].y, s2[1].x + s2[1].y, s2[2].x + s2[2].y, s2[3].x + s2[3].y);
            qo_ = fmaxf(qo_ - so_ * so_ * (g < 2 ? 1.f / 32.f : 1.f / 16.f), 0.f);
            *(LAS f32x2*)(L + L_ZS + (w * 64 + (g ^ (2 * wo)) * 16 + c) * 8) = f32x2{so_, qo_};
            BAR();
            if (w == 0) {
                f32x2 pr[4];
#pragma unroll
                for (int ww = 0; ww < 4; ++ww) pr[ww] = *(LAS f32x2*)(L + L_ZS + (ww * 64) * 8 + l8);
                float tot = (pr[0].x + pr[1].x) + (pr[2].x + pr[3].x);
                float mu = tot * (1.f / 96.f), mm = 0.f;
#pragma unroll
                for (int ww = 0; ww < 4; ++ww) {
                    bool big = (g ^ (2 * (ww & 1))) < 2;
                    float d = pr[ww].x * (big ? 1.f / 32.f : 1.f / 16.f) - mu;
                    mm += pr[ww].y + (big ? 32.f : 16.f) * d * d;
                }
                st64s<SC1>(canon(tot, mm), rs, l8, O_STZ + layer * SZ_ST + wg * 512);
            }
            STAMP(7);
        }
        xb_fill(0);
        g1_steps(1, 0, Z1SPLIT, Z1SPLIT);
        STAMP(8);
        {
            v2u sa, sb;
            poll2(rs, g * 1024 + c * 8, O_STZ + layer * SZ_ST + (rb * 8 * 64 + w * 16) * 8, sa, sb);
            f32x2 st = merge_stats<96>(sa, sb);
            if (g == 0) *(LAS f32x2*)(L + L_ZN + (w * 16 + c) * 8) = f32x2{st.y, -st.x * st.y};
        }
        BAR();
        STAMP(9);
        {
            f32x2 st[4];
#pragma unroll
            for (int t = 0; t < 4; ++t) st[t] = *(LAS f32x2*)(L + L_ZN + ((t ^ (2 * wo)) * 16 + c) * 8);
            v2u a[6];
#pragma unroll
            for (int t = 0; t < 6; ++t) {
                f32x4 d = z2[t] * st[t & 3].x + st[t & 3].y;
                a[t] = v2u{pk2(f32x2{d[0], d[1]}), pk2(f32x2{d[2], d[3]})};
            }
            f32x4 tr[6];
#pragma unroll
            for (int t = 0; t < 6; ++t) mf16_ini(tr[t], a[t], ident);
            MFENCE6(tr[0], tr[1], tr[2], tr[3], tr[4], tr[5]);
            v2u T[6];
#pragma unroll
            for (int t = 0; t < 6; ++t) T[t] = v2u{pk(tr[t][0], tr[t][1]), pk(tr[t][2], tr[t][3])};
            const unsigned zo = O_Z2F + layer * SZ_Z + (6 * j * 64 + 2 * rb) * 1024;
            st128s<SC1>(v4u{T[0].x, T[0].y, T[1].x, T[1].y}, rs, l16, zo + (w * 64 + wo) * 1024);
            st128s<SC1>(v4u{T[2].x, T[2].y, T[3].x, T[3].y}, rs, l16, zo + (w * 64 + (wo ^ 1)) * 1024);
            st128s<SC1>(v4u{T[4].x, T[4].y, T[5].x, T[5].y}, rs, l16, zo + (ftb * 64 + wo) * 1024);
        }
        SB();
        xb_fill(Z1SPLIT);
        g1_steps(1, Z1SPLIT, 20, 20);
        DRAIN();
        BAR();
        if (tid < 32) __hip_atomic_fetch_add(cnt + (CNT_B + (tid * 8 + j) * 4 + (rb >> 3)) * 32, 1u, RLX_AGENT);
        STAMP(10);
        SB();
        {
            unsigned w3s = O_W3F + (j * 120 + w * 30) * 1024; OPQ(w3s);
            unsigned w3l = ldsbase + L_W3 + w * 30 * 1024; OPQ(w3l);
#pragma unroll
            for (int i = 0; i < 30; ++i) glds16(rs, w3l + i * 1024, l16, w3s + i * 1024);
        }
        MFENCE6(aa[0], aa[1], aa[2], aa[3], aa[4], aa[5]);
        RELANE();
        {
            f32x4 csa = *(LAS f32x4*)(L + L_CS + piperm(w, 4 * g) * 4), csb = *(LAS f32x4*)(L + L_CS + piperm(ftb, 4 * g) * 4);
            f32x2 sx[4];
#pragma unroll
            for (int i = 0; i < 4; ++i) sx[i] = *(LAS f32x2*)(L + L_SX + ((i ^ (2 * wo)) * 16 + c) * 8);
#pragma unroll
            for (int t = 0; t < 6; ++t) {
                f32x4 cs = t < 4 ? csa : csb;
                f32x4 v = aa[t] * sx[t & 3].x + cs * sx[t & 3].y;
                v2u o = {pk2(gelu2(f32x2{v[0], v[1]})), pk2(gelu2(f32x2{v[2], v[3]}))};
                *(LAS v2u*)(L + L_Z1 + (((t & 3) ^ (2 * wo)) * 6 + (t < 4 ? w : ftb)) * 512 + l8) = o;
            }
        }
        wait_cnt(cnt + (CNT_B + wg * 4 + w) * 32, 8u * (layer + 1));
        STAMP(11);
        RELANE();
        {
            unsigned zo[3];
            int ppe[3];
#pragma unroll
            for (int pp = 0; pp < 3; ++pp) {
                int e = pp + rot; e = e >= 3 ? e - 3 : e; ppe[pp] = e;
                zo[pp] = O_Z2F + layer * SZ_Z + ((6 * j + 2 * e) * 64 + 16 * w) * 1024; OPQ(zo[pp]);
            }
            v4u z[96];
#define ZOFF(i) (zo[(i) >> 5] + (((i) & 1) * 64 + (((i) >> 1) & 15)) * 1024)
#pragma unroll
            for (int i = 0; i < ZWIN; ++i) z[i] = ld128s<SC1>(rs, l16, ZOFF(i));
            SB();
            LAS unsigned char* R = L + L_RED + l16;
            f32x4 own[2];
            f32x4 part[6]; v2u z1[2];
            v4u uo4;
            const f32x4 bs4 = {bsp, bsp, bsp, bsp};
            const unsigned uo = O_UF + layer * SZ_Z + ((rb * 4 + w) * 24 + 3 * j) * 1024;
            auto epi = [&](int step, int pe) {
                if (step < 6) part[step] = *(LAS f32x4*)(R + (w * 6 + step) * 1024);
                else if (step < 8) z1[step - 6] = *(LAS v2u*)(L + L_Z1 + (w * 6 + 2 * pe + step - 6) * 512 + l8);
                else if (step == 10) own[0] += part[0];
                else if (step == 11) own[1] += part[1];
                else if (step == 12) part[2] += part[4];
                else if (step == 13) part[3] += part[5];
                else if (step == 14) own[0] += part[2];
                else if (step == 15) own[1] += part[3];
                else if (step == 16) uo4[0] = pk2(up2(z1[0].x) * f32x2{own[0][0], own[0][1]});
                else if (step == 17) uo4[1] = pk2(up2(z1[0].y) * f32x2{own[0][2], own[0][3]});
                else if (step == 18) uo4[2] = pk2(up2(z1[1].x) * f32x2{own[1][0], own[1][1]});
                else if (step == 19) uo4[3] = pk2(up2(z1[1].y) * f32x2{own[1][2], own[1][3]});
                else if (step == 20) st128s<SC1>(uo4, rs, l16, uo + pe * 1024);
            };
            f32x4 acc[2][2][4];
#pragma unroll
            for (int pp = 0; pp < 3; ++pp) {
                const int A = pp & 1;
#pragma unroll
                for (int kk = 0; kk < 16; ++kk) {
#pragma unroll
                    for (int h = 0; h < 2; ++h) {
                        const int i = pp * 32 + kk * 2 + h;
                        const int sl = kk * 2 + h;
#pragma unroll
                        for (int t = 0; t < 4; ++t) {
                            if (kk == 0) { if (t == 0) mf_inic_a(acc[A][h][t], z[i], Bw[t][kk], bs4); else mf_ini_a(acc[A][h][t], z[i], Bw[t][kk]); }
                            else mf_acc_a(acc[A][h][t], z[i], Bw[t][kk]);
                            if (t == 1 && pp > 0) {
                                if (sl >= 1 && sl <= 3) {
                                    LAS unsigned char* dst = R + (((w + sl) & 3) * 6 + (sl - 1) * 2) * 1024;
                                    *(LAS f32x4*)(dst) = acc[A ^ 1][0][sl];
                                    *(LAS f32x4*)(dst + 1024) = acc[A ^ 1][1][sl];
                                }
                                if (sl == 4) { own[0] = acc[A ^ 1][0][0]; own[1] = acc[A ^ 1][1][0]; }
                                if (sl == 5) BAR();
                                if (sl >= 6 && sl <= 26) epi(sl - 6, ppe[pp - 1]);
                                if (sl == 30) __builtin_amdgcn_s_barrier();
                                SB();
                            }
                        }
                        if (i + ZWIN < 96) z[i + ZWIN] = ld128s<SC1>(rs, l16, ZOFF(i + ZWIN));
                        SB();
                    }
                }
            }
            MFENCE8(acc[0][0][0], acc[0][0][1], acc[0][0][2], acc[0][0][3], acc[0][1][0], acc[0][1][1], acc[0][1][2], acc[0][1][3]);
#pragma unroll
            for (int t = 1; t < 4; ++t) {
                LAS unsigned char* dst = R + (((w + t) & 3) * 6 + (t - 1) * 2) * 1024;
                *(LAS f32x4*)(dst) = acc[0][0][t];
                *(LAS f32x4*)(dst + 1024) = acc[0][1][t];
            }
            own[0] = acc[0][0][0]; own[1] = acc[0][1][0];
            STAMP(12);
            BAR();
#pragma unroll
            for (int st_ = 0; st_ <= 20; ++st_) epi(st_, ppe[2]);
        }
        DRAIN();
        if (l < 8) __hip_atomic_fetch_add(cnt + (CNT_C + (rb * 8 + l) * 4 + 2 * (w >> 1) + (j >> 2)) * 32, 1u, RLX_AGENT);
        STAMP(13);
        {
            RELANE();
            LAS unsigned char* wl = L + L_W3 + 12 * wo * 1024 + l16;
            LAS unsigned char* wl2 = wl + 3 * 24576;
#define W3RD(kk, t5) (*(LAS v4u*)(((t5) < 3 ? wl : wl2) + ((t5) % 3) * 24576 + (kk) * 1024))
            v4u wv[2][5];
#pragma unroll
            for (int t5 = 0; t5 < 5; ++t5) wv[0][t5] = W3RD(0, t5);
            wait_cnt(cnt + (CNT_C + wg * 4 + w) * 32, 8u * (layer + 1));
            STAMP(14);
            v4u ub[2][12];
            unsigned uo0 = O_UF + layer * SZ_Z + ((rb * 4 + w) * 24 + 12 * wo) * 1024, uo1 = O_UF + layer * SZ_Z + ((rb * 4 + (w ^ 1)) * 24 + 12 * wo) * 1024;
            OPQ(uo0); OPQ(uo1);
#pragma unroll
            for (int kk = 0; kk < UWIN; ++kk) {
                ub[0][kk] = ld128s<SC1>(rs, l16, uo0 + kk * 1024);
                ub[1][kk] = ld128s<SC1>(rs, l16, uo1 + kk * 1024);
            }
            f32x4 acc[5][2];
            SB();
#pragma unroll
            for (int kk = 0; kk < 12; ++kk) {
#pragma unroll
                for (int t5 = 0; t5 < 5; ++t5) {
                    if (kk == 0) mf_ini(acc[t5][0], wv[kk & 1][t5], ub[0][kk]); else mf_acc(acc[t5][0], wv[kk & 1][t5], ub[0][kk]);
                    if (kk + 1 < 12) wv[(kk + 1) & 1][t5] = W3RD(kk + 1, t5);
                    SB();
                    if (kk == 0) mf_ini(acc[t5][1], wv[kk & 1][t5], ub[1][kk]); else mf_acc(acc[t5][1], wv[kk & 1][t5], ub[1][kk]);
                    if (t5 < 2 && kk + UWIN < 12) ub[t5][kk + UWIN] = ld128s<SC1>(rs, l16, (t5 ? uo1 : uo0) + (kk + UWIN) * 1024);
                    SB();
                }
                if (kk == 2) { __builtin_amdgcn_s_barrier(); SB(); }
            }
            MFENCE2(acc[0][0], acc[0][1]);
            MFENCE8(acc[1][0], acc[1][1], acc[2][0], acc[2][1], acc[3][0], acc[3][1], acc[4][0], acc[4][1]);
            STAMP(15);
            RELANE();
            LAS unsigned char* R = L + L_RED + l16;
#pragma unroll
            for (int t5 = 0; t5 < 5; ++t5) *(LAS f32x4*)(R + ((w ^ 1) * 5 + t5) * 1024) = acc[t5][1];
            BAR();
            f32x4 ot[5];
#pragma unroll
            for (int t5 = 0; t5 < 5; ++t5) ot[t5] = *(LAS f32x4*)(R + (w * 5 + t5) * 1024);
#pragma unroll
            for (int t5 = 0; t5 < 5; ++t5) {
                f32x4 sv = acc[t5][0] + ot[t5];
                xs[2 * t5] = pk2(up2(xs[2 * t5]) + f32x2{sv[0], sv[1]});
                xs[2 * t5 + 1] = pk2(up2(xs[2 * t5 + 1]) + f32x2{sv[2], sv[3]});
            }
            if (layer == 0) {
                f32x2 ps = slice_stats(xs);
                if (g == 0) st64s<SC1>(canon(ps.x, ps.y), rs, c * 8, O_STX + 2 * SZ_ST + (rb * 8 * 64 + w * 16) * 8 + j * 512);
            }
            STAMP(16);
        }
    }
    {
        const int cc0 = 10 * j + g, cc1 = cc0 + 4, cc2 = 10 * j + 8 + (g >> 1);
        u16* orow = p.out + (size_t)(rb * 64 + piperm(w, c)) * D;
        *(v4u*)(orow + cc0 * 8) = v4u{xs[0], xs[1], xs[2], xs[3]};
        *(v4u*)(orow + cc1 * 8) = v4u{xs[4], xs[5], xs[6], xs[7]};
        *(v2u*)(orow + cc2 * 8 + (g & 1) * 4) = v2u{xs[8], xs[9]};
    }
}

extern "C" void kernel_launch(void* const* d_in, const int* in_sizes, int n_in,
                              void* d_out, int out_size, void* d_ws, size_t ws_size,
                              hipStream_t stream) {
    Params p;
    p.q = (const u16*)d_in[0]; p.k = (const u16*)d_in[1]; p.v = (const u16*)d_in[2];
    p.shk = (const u16*)d_in[3]; p.ssmk = (const u16*)d_in[4]; p.ssmD = (const u16*)d_in[5];
    p.w_in = (const u16*)d_in[7]; p.w_sp = (const u16*)d_in[8]; p.b_sp = (const u16*)d_in[9]; p.w_out = (const u16*)d_in[10];
    p.out = (u16*)d_out; p.ws = (unsigned char*)d_ws;
    (void)hipFuncSetAttribute((const void*)k_main, hipFuncAttributeMaxDynamicSharedMemorySize, L_END);
    k_pre<<<PRE_N, 256, 0, stream>>>(p);
    k_main<<<256, 256, L_END, stream>>>(p);
}
```

```cpp
#include <hip/hip_runtime.h>
#include <stdint.h>

typedef unsigned short u16;
typedef unsigned v4u __attribute__((ext_vector_type(4)));
typedef unsigned v2u __attribute__((ext_vector_type(2)));
typedef float f32x4 __attribute__((ext_vector_type(4)));
typedef float f32x2 __attribute__((ext_vector_type(2)));
typedef short s16x4 __attribute__((ext_vector_type(4)));
typedef __bf16 bf2v __attribute__((ext_vector_type(2)));
#define GAS __attribute__((address_space(1)))
#define LAS __attribute__((address_space(3)))
typedef GAS unsigned gu32;
#define RLX_AGENT __ATOMIC_RELAXED, __HIP_MEMORY_SCOPE_AGENT
#define DRAIN() asm volatile("s_waitcnt vmcnt(0)" ::: "memory")
#define WAITV(n) asm volatile("s_waitcnt vmcnt(%0)" :: "i"(n) : "memory")
#define WAITL(n) __builtin_amdgcn_s_waitcnt(0xC07F | ((n) << 8))
#define CBAR() asm volatile("" ::: "memory")
#define DI __device__ __forceinline__
#define SB() __builtin_amdgcn_sched_barrier(0)
#define OPQ(x) asm volatile("" : "+s"(x))
#define BAR() do { asm volatile("s_waitcnt lgkmcnt(0)" ::: "memory"); __builtin_amdgcn_s_barrier(); asm volatile("" ::: "memory"); } while (0)

constexpr int S = 2048, D = 640, F = 1536, FH = 768;
constexpr float EPS = 1e-5f;
constexpr unsigned O_CNT = 0;
constexpr unsigned CNT_B = 0, CNT_C = 1024, CNT_D = 2048, CNT_A = 3072, CNT_W = 3328, CNT_N = 3584;
constexpr unsigned O_STZ = O_CNT + CNT_N * 128;
constexpr unsigned SZ_ST = 32 * 8 * 64 * 8;
constexpr unsigned O_STX = O_STZ + 2 * SZ_ST;
constexpr unsigned O_CS = O_STX + 4 * SZ_ST;
constexpr unsigned O_KVT = O_CS + 20 * 1536 * 4;
constexpr unsigned SZ_X = S * D * 2;
constexpr unsigned O_YF = O_KVT + SZ_X;
constexpr unsigned O_XF = O_YF + SZ_X;
constexpr unsigned O_W1F = O_XF + 2 * SZ_X;
constexpr unsigned O_W3F = O_W1F + D * F * 2;
constexpr unsigned SZ_Z = S * FH * 2;
constexpr unsigned O_Z2F = O_W3F + FH * D * 2;
constexpr unsigned O_UF = O_Z2F + 2 * SZ_Z;
constexpr unsigned WS_END = O_UF + 2 * SZ_Z;

DI float bf2f(u16 b) { return __uint_as_float(((unsigned)b) << 16); }
DI float lo16(unsigned u) { return __uint_as_float(u << 16); }
DI float hi16(unsigned u) { return __uint_as_float(u & 0xffff0000u); }
DI unsigned pk2(f32x2 v) { bf2v r = __builtin_convertvector(v, bf2v); return __builtin_bit_cast(unsigned, r); }
DI unsigned pk(float a, float b) { return pk2(f32x2{a, b}); }
DI float rbf(float a) { return lo16(pk(a, a)); }
DI f32x2 up2(unsigned u) { return f32x2{lo16(u), hi16(u)}; }
DI f32x2 rbf2(f32x2 v) { return up2(pk2(v)); }
DI void mf_acc(f32x4& d, v4u a, v4u b) { asm volatile("v_mfma_f32_16x16x32_bf16 %0, %1, %2, %0" : "+v"(d) : "v"(a), "v"(b)); }
DI void mf_ini(f32x4& d, v4u a, v4u b) { asm volatile("v_mfma_f32_16x16x32_bf16 %0, %1, %2, 0" : "=&v"(d) : "v"(a), "v"(b)); }
DI void mf_acc_a(f32x4& d, v4u a, v4u b) { asm volatile("v_mfma_f32_16x16x32_bf16 %0, %1, %2, %0" : "+v"(d) : "v"(a), "a"(b)); }
DI void mf_ini_a(f32x4& d, v4u a, v4u b) { asm volatile("v_mfma_f32_16x16x32_bf16 %0, %1, %2, 0" : "=&v"(d) : "v"(a), "a"(b)); }
DI void mf_inic_a(f32x4& d, v4u a, v4u b, f32x4 c) { asm volatile("s_nop 1\n\tv_mfma_f32_16x16x32_bf16 %0, %1, %2, %3" : "=&v"(d) : "v"(a), "a"(b), "v"(c)); }
DI void mf_ini_n(f32x4& d, v4u a, v4u b) { asm volatile("s_nop 1\n\tv_mfma_f32_16x16x32_bf16 %0, %1, %2, 0" : "=&v"(d) : "v"(a), "v"(b)); }
DI void mf16_ini(f32x4& d, v2u a, v2u b) { asm volatile("s_nop 1\n\tv_mfma_f32_16x16x16_bf16 %0, %1, %2, 0" : "=&v"(d) : "v"(a), "v"(b)); }
#define MFENCE2(a, b) asm volatile("s_nop 7" : "+v"(a), "+v"(b))
#define MFENCE4(a, b, c, d) asm volatile("s_nop 7" : "+v"(a), "+v"(b), "+v"(c), "+v"(d))
#define MFENCE6(a, b, c, d, e, f) asm volatile("s_nop 7" : "+v"(a), "+v"(b), "+v"(c), "+v"(d), "+v"(e), "+v"(f))
#define MFENCE8(a, b, c, d, e, f, g, h) asm volatile("s_nop 7" : "+v"(a), "+v"(b), "+v"(c), "+v"(d), "+v"(e), "+v"(f), "+v"(g), "+v"(h))
DI int piperm(int tile, int c) { return 32 * (tile >> 1) + 8 * (c >> 2) + 4 * (tile & 1) + (c & 3); }
typedef __amdgpu_buffer_rsrc_t rsrc_t;
DI rsrc_t mkrs(const void* p, unsigned bytes) { return __builtin_amdgcn_make_buffer_rsrc((void*)p, 0, (int)bytes, 0x00020000); }
constexpr int SC1 = 16;
template <int AUX> DI v4u ld128s(rsrc_t rs, unsigned voff, unsigned soff) { return __builtin_amdgcn_raw_buffer_load_b128(rs, voff, soff, AUX); }
template <int AUX> DI v2u ld64s(rsrc_t rs, unsigned voff, unsigned soff) { return __builtin_amdgcn_raw_buffer_load_b64(rs, voff, soff, AUX); }
template <int AUX> DI void st128s(v4u v, rsrc_t rs, unsigned voff, unsigned soff) { __builtin_amdgcn_raw_buffer_store_b128(v, rs, voff, soff, AUX); }
template <int AUX> DI void st64s(v2u v, rsrc_t rs, unsigned voff, unsigned soff) { __builtin_amdgcn_raw_buffer_store_b64(v, rs, voff, soff, AUX); }
#define LDF(base, idx) ld128s<SC1>(rs, l16 + ((idx) & 3) * 1024, (base) + ((idx) >> 2) * 4096)
DI v2u trrd(LAS unsigned char* a) { return __builtin_bit_cast(v2u, __builtin_amdgcn_ds_read_tr16_b64_v4i16((LAS s16x4*)a)); }

struct Params {
    const u16 *q, *k, *v, *shk, *ssmk, *ssmD, *w_in, *w_sp, *b_sp, *w_out;
    u16* out;
    unsigned char* ws;
};

constexpr int PRE_KV = 320, PRE_CS = 96, PRE_Z = 24;
constexpr int PRE_N = PRE_KV + PRE_CS + PRE_Z;
DI void pre_body(const Params& p, LAS unsigned char* M) {
    int b = blockIdx.x;
    const int tid = threadIdx.x;
    unsigned char* ws = p.ws;
    const rsrc_t rw = mkrs(p.ws, WS_END);
    if (b < PRE_KV) {
        const int cg = b % 10, rt = b / 10;
        const int ch0 = cg * 64 + (tid & 7) * 8, rl = (tid >> 3) * 2, s0 = rt * 64 + rl;
        v4u kk[5], vv[2];
#pragma unroll
        for (int i = 0; i < 5; ++i) {
            int s = s0 - 3 + i;
            kk[i] = s >= 0 ? *(const v4u*)(p.k + (size_t)s * D + ch0) : v4u{0, 0, 0, 0};
        }
#pragma unroll
        for (int i = 0; i < 2; ++i) vv[i] = *(const v4u*)(p.v + (size_t)(s0 + i) * D + ch0);
        v4u sh[4];
#pragma unroll
        for (int c = 0; c < 4; ++c) sh[c] = *(const v4u*)(p.shk + (size_t)(ch0 + 2 * c) * 4);
        const int pos = (rl & ~7) + 6 - (rl & 6);
#pragma unroll
        for (int c2 = 0; c2 < 4; ++c2) {
            f32x2 w0 = {lo16(sh[c2].x), lo16(sh[c2].z)}, w1 = {hi16(sh[c2].x), hi16(sh[c2].z)}, w2 = {lo16(sh[c2].y), lo16(sh[c2].w)}, w3 = {hi16(sh[c2].y), hi16(sh[c2].w)};
            f32x2 kf[5];
#pragma unroll
            for (int i = 0; i < 5; ++i) kf[i] = up2(kk[i][c2]);
            f32x2 r[2];
#pragma unroll
            for (int i = 0; i < 2; ++i) {
                f32x2 a = rbf2(kf[i] * w3);
                a = rbf2(a + rbf2(kf[i + 1] * w2));
                a = rbf2(a + rbf2(kf[i + 2] * w1));
                a = rbf2(a + rbf2(kf[i + 3] * w0));
                r[i] = a * up2(vv[i][c2]);
            }
            *(LAS unsigned*)(M + ((tid & 7) * 8 + 2 * c2) * 144 + pos * 2) = pk(r[1].x, r[0].x);
            *(LAS unsigned*)(M + ((tid & 7) * 8 + 2 * c2 + 1) * 144 + pos * 2) = pk(r[1].y, r[0].y);
        }
        __syncthreads();
#pragma unroll
        for (int i = 0; i < 2; ++i) {
            int id = i * 256 + tid, ch = id >> 3, part = id & 7;
            v4u o = *(LAS v4u*)(M + ch * 144 + part * 16);
            st128s<SC1>(o, rw, ch * (S * 2) + part * 16, O_KVT + cg * 64 * (S * 2) + rt * 128);
        }
        return;
    }
    b -= PRE_KV;
    if (b < PRE_CS) {
        const int cgp = b % 24, qr = b / 24, c8 = tid & 7, rg = tid >> 3;
        f32x2 a[4] = {};
        v4u x[5];
#pragma unroll
        for (int i = 0; i < 5; ++i) x[i] = *(const v4u*)(p.w_in + (size_t)(qr * 160 + rg * 5 + i) * F + cgp * 64 + c8 * 8);
#pragma unroll
        for (int i = 0; i < 5; ++i)
#pragma unroll
            for (int e = 0; e < 4; ++e) a[e] += up2(x[i][e]);
        LAS float* sf = (LAS float*)M;
#pragma unroll
        for (int e = 0; e < 4; ++e) { sf[rg * 65 + c8 * 8 + 2 * e] = a[e].x; sf[rg * 65 + c8 * 8 + 2 * e + 1] = a[e].y; }
        __syncthreads();
        if (tid < 64) {
            float s_ = 0.f;
#pragma unroll
            for (int r = 0; r < 32; ++r) s_ += sf[r * 65 + tid];
            ((float*)(ws + O_CS))[qr * F + cgp * 64 + tid] = s_;
        }
        return;
    }
    b -= PRE_CS;
    {
        for (int i = b * 256 + tid; i < (int)CNT_N; i += PRE_Z * 256) { *(v4u*)(ws + O_CNT + i * 128) = v4u{0, 0, 0, 0}; *(v4u*)(ws + O_CNT + i * 128 + 16) = v4u{0, 0, 0, 0}; }
        for (int i = b * 256 + tid; i < (int)(6 * SZ_ST / 16); i += PRE_Z * 256) *(v4u*)(ws + O_STZ + i * 16) = v4u{~0u, ~0u, ~0u, ~0u};
    }
}

__global__ void __launch_bounds__(256) k_pre(Params p) {
    __shared__ __attribute__((aligned(16))) unsigned char sm[64 * 144];
    pre_body(p, (LAS unsigned char*)sm);
}

constexpr int L_XA = 0;
constexpr int L_W3 = 0;
constexpr int L_Z1 = 122880;
constexpr int L_RED = L_Z1 + 12288;
constexpr int L_SX = L_RED + 24576;
constexpr int L_ZS = L_SX + 512;
constexpr int L_ZN = L_ZS + 2048;
constexpr int L_CS = L_ZN + 512;
constexpr int L_END = L_CS + 768;
static_assert(L_END <= 163840, "lds");
constexpr int CV_CPX = 4416, CV_CPY = 2368;
constexpr int CV_CHY = 4 * CV_CPY + 32 * 160 + 512;
constexpr int CV_WREG = 2 * CV_CHY;
constexpr int CV_OUT = 4 * CV_WREG;
constexpr int CV_WT = CV_OUT + 8 * 2176;
static_assert(CV_WT + 4 * 5120 <= L_SX && CV_CHY % 256 == 0 && CV_WT % 16 == 0, "conv lds");
constexpr unsigned SPIN_CAP = 1u << 18;

DI void wait_cnt(gu32* c, unsigned want) {
    for (unsigned sp = 0; sp < SPIN_CAP; ++sp) {
        if (__hip_atomic_load(c, RLX_AGENT) >= want) break;
    }
    __builtin_amdgcn_fence(__ATOMIC_ACQUIRE, "wavefront");
    CBAR();
}
DI float rsum4(float v) {
    unsigned u = __float_as_uint(v);
    auto a = __builtin_amdgcn_permlane16_swap(u, u, false, false);
    v = __uint_as_float(a[0]) + __uint_as_float(a[1]);
    u = __float_as_uint(v);
    auto b = __builtin_amdgcn_permlane32_swap(u, u, false, false);
    return __uint_as_float(b[0]) + __uint_as_float(b[1]);
}
DI float tred4(float a, float b, float c, float d) {
    auto p = __builtin_amdgcn_permlane16_swap(__float_as_uint(a), __float_as_uint(b), false, false);
    float ab = __uint_as_float(p[0]) + __uint_as_float(p[1]);
    auto q = __builtin_amdgcn_permlane16_swap(__float_as_uint(c), __float_as_uint(d), false, false);
    float cd = __uint_as_float(q[0]) + __uint_as_float(q[1]);
    auto r = __builtin_amdgcn_permlane32_swap(__float_as_uint(ab), __float_as_uint(cd), false, false);
    return __uint_as_float(r[0]) + __uint_as_float(r[1]);
}
DI f32x2 gelu2(f32x2 x) {
    constexpr float C1 = -2.f * 0.7978845608f * 0.044715f * 1.4426950409f, C0 = -2.f * 0.7978845608f * 1.4426950409f;
    f32x2 t = x * (x * x * C1 + C0);
    f32x2 e = {__builtin_amdgcn_exp2f(t.x), __builtin_amdgcn_exp2f(t.y)};
    e = e + 1.f;
    f32x2 r = {__builtin_amdgcn_rcpf(e.x), __builtin_amdgcn_rcpf(e.y)};
    return x * r;
}
DI f32x2 fin_stats(float mean, float var) {
    float sd = rbf(__builtin_amdgcn_sqrtf(rbf(rbf(var) + EPS)));
    return f32x2{rbf(mean), __builtin_amdgcn_rcpf(sd)};
}
template <int N> DI f32x2 merge_stats(v2u ua, v2u ub) {
    f32x2 a = __builtin_bit_cast(f32x2, ua), b = __builtin_bit_cast(f32x2, ub);
    float mean = rsum4(a.x + b.x) * (1.f / (8 * N));
    float da = a.x * (1.f / N) - mean, db = b.x * (1.f / N) - mean;
    float m2 = rsum4(a.y + b.y + (float)N * (da * da + db * db));
    return fin_stats(mean, m2 * (1.f / (8 * N)));
}
DI v2u canon(float a, float b) {
    v2u o = {__float_as_uint(a), __float_as_uint(b)};
    if (o.x == ~0u) o.x = 0x7fc00000u;
    if (o.y == ~0u) o.y = 0x7fc00000u;
    return o;
}
DI f32x2 slice_stats(const unsigned (&o)[10]) {
    f32x2 v[10];
#pragma unroll
    for (int e = 0; e < 10; ++e) v[e] = up2(o[e]);
    f32x2 s = ((v[0] + v[1]) + (v[2] + v[3])) + ((v[4] + v[5]) + (v[6] + v[7])) + (v[8] + v[9]);
    float sum = rsum4(s.x + s.y);
    float mu = sum * (1.f / 80.f);
    f32x2 q = {0.f, 0.f};
#pragma unroll
    for (int e = 0; e < 10; ++e) { f32x2 d = v[e] - mu; q += d * d; }
    return f32x2{sum, rsum4(q.x + q.y)};
}
DI f32x2 slice_stats_f(const f32x2 (&v)[10]) {
    f32x2 s = ((v[0] + v[1]) + (v[2] + v[3])) + ((v[4] + v[5]) + (v[6] + v[7])) + (v[8] + v[9]);
    float sum = rsum4(s.x + s.y);
    float mu = sum * (1.f / 80.f);
    f32x2 q = {0.f, 0.f};
#pragma unroll
    for (int e = 0; e < 10; ++e) { f32x2 d = v[e] - mu; q += d * d; }
    return f32x2{sum, rsum4(q.x + q.y)};
}
DI void poll2(rsrc_t rs, unsigned voff, unsigned so, v2u& sa, v2u& sb) {
    for (unsigned sp = 0; sp < SPIN_CAP; ++sp) {
        CBAR();
        sa = ld64s<SC1>(rs, voff, so); sb = ld64s<SC1>(rs, voff, so + 512);
        bool ok = sa.x != ~0u && sa.y != ~0u && sb.x != ~0u && sb.y != ~0u;
        if (__all(ok)) break;
    }
}
DI void glds16(rsrc_t rs, unsigned lds_addr, unsigned voff, unsigned soff) {
    asm volatile("s_mov_b32 m0, %0\n\ts_nop 0\n\tbuffer_load_dwordx4 %1, %2, %3 offen lds" :: "s"(lds_addr), "v"(voff), "s"(rs), "s"(soff) : "memory");
}
DI void glds16c(rsrc_t rs, unsigned lds_addr, unsigned voff, unsigned soff) {
    asm volatile("s_mov_b32 m0, %0\n\ts_nop 0\n\tbuffer_load_dwordx4 %1, %2, %3 offen sc1 lds" :: "s"(lds_addr), "v"(voff), "s"(rs), "s"(soff) : "memory");
}
constexpr int NPRE1 = 10, NPRE = 26;
constexpr int UWIN = 4;
constexpr int Z1SPLIT = 10;

__global__ void __launch_bounds__(256, 1) k_main(Params p) {
    extern __shared__ __attribute__((aligned(16))) unsigned char smem[];
    LAS unsigned char* L = (LAS unsigned char*)smem;
    const int tid = threadIdx.x, w = __builtin_amdgcn_readfirstlane(tid >> 6);
    int l = tid & 63;
    int c = l & 15, g = l >> 4;
    const int bx = blockIdx.x & 7, bi = blockIdx.x >> 3;
#define MAP 1
    const int rb = (bx >> MAP) * (4 << MAP) + (bi >> (3 - MAP)), j = (bx & ((1 << MAP) - 1)) * (8 >> MAP) + (bi & ((8 >> MAP) - 1));
    const int wg = rb * 8 + j;
    const rsrc_t rs = mkrs(p.ws, WS_END);
    gu32* cnt = (gu32*)(p.ws + O_CNT);
    unsigned l16 = l * 16, l8 = l * 8;
#define OPQ10(a) asm volatile("" : "+v"(a[0]), "+v"(a[1]), "+v"(a[2]), "+v"(a[3]), "+v"(a[4]), "+v"(a[5]), "+v"(a[6]), "+v"(a[7]), "+v"(a[8]), "+v"(a[9]))
#define RELANE() do { asm volatile("" : "+v"(l)); c = l & 15; g = l >> 4; l16 = l * 16; l8 = l * 8; } while (0)
    const int wo = w & 1;
    const int ftb = 4 + (w >> 1);
    const int rot = 0;
    const unsigned ldsbase = (unsigned)(size_t)smem;
    const unsigned w1a_ = O_W1F + (6 * j + w) * 20 * 1024, w1b_ = O_W1F + (6 * j + ftb) * 20 * 1024;
#define W1BASES() unsigned w1a = w1a_, w1b = w1b_; OPQ(w1a); OPQ(w1b)
#define W1LD(idx) LDF((((idx) & 1) ? w1b : w1a) + ((idx) < 40 ? 48 * 20 * 1024 : 0), ((idx) % 40) >> 1)

    int layer = 0;
    unsigned xs[10], ys[10];
    v4u Bw[4][16];
    v4u raw[64];
    const rsrc_t rsp = mkrs(p.w_sp, S * S * 2);
    const unsigned spb_ = (unsigned)(rb * 64) * (S * 2) + 1024 * w;
#define RAWLD(i0) do { const int r_ = (i0) & 15; \
        raw[i0] = ld128s<0>(rsp, l16, spt[(i0) >> 4] + (unsigned)(8 * (r_ >> 2) + (r_ & 3)) * (S * 2)); } while (0)
    auto tpose = [&](int t, int bo) __attribute__((always_inline)) {
        LAS unsigned char* T = L + bo + w * 16384 + c * 1024;
        const unsigned tb = ldsbase + bo + w * 16384;
#pragma unroll
        for (int r = 0; r < 16; ++r) {
            const unsigned a_ = tb + ((l ^ r) << 4);
            asm volatile("ds_write_b128 %0, %1 offset:%2" :: "v"(a_), "a"(raw[16 * t + r]), "i"(r * 1024) : "memory");
        }
#pragma unroll
        for (int kk = 0; kk < 16; ++kk) Bw[t][kk] = *(LAS v4u*)(T + (((4 * kk + g) ^ c) << 4));
        SB();
    };
    float cspart[4];
    {
        const bool real = blockIdx.x < 240;
        const int cu = real ? blockIdx.x : blockIdx.x - 240;
        const bool isX = cu < 160;
        const int chA = isX ? cu * 4 + w : (cu - 160) * 8 + 2 * w;
        const int CPY = isX ? CV_CPX : CV_CPY;
        const int chB = __builtin_amdgcn_readfirstlane(isX ? chA : chA + 1);
        LAS unsigned char* RG = L + w * CV_WREG;
        const rsrc_t rsk = mkrs(p.ssmk, D * S * 2);
        const int seg1 = isX ? 0 : CV_CHY;
        const unsigned rgl = ldsbase + w * CV_WREG;
        v4u ka[4]; v2u kb[4];
#pragma unroll
        for (int r = 0; r < 4; ++r) {
            const unsigned so = (r < 2 ? chA : chB) * 4096 + (isX ? r : (r & 1)) * 1024;
            ka[r] = ld128s<0>(rsk, l16, so);
            kb[r] = ld64s<0>(rsk, l16, so + 16);
        }
        float Dv0 = bf2f(p.ssmD[chA]), Dv1 = bf2f(p.ssmD[chB]);
        SB();
#pragma unroll
        for (int i = 0; i < 6; ++i) {
            const int sl = (isX ? i : i % 3) * 64 + l;
            const int blk = (sl * 6554) >> 16, chk = sl - blk * 10;
            const unsigned vo = blk * 128 + (chk > 7 ? 7 : chk) * 16;
            const unsigned dst = rgl + 4 * CPY + 2560 + (isX ? i * 1024 : (i / 3) * CV_CHY + (i % 3) * 1024);
            glds16(rs, dst, vo, O_KVT + (i < 3 ? chA : chB) * 4096);
        }
#pragma unroll
        for (int r = 0; r < 4; ++r) {
            const unsigned so = (r < 2 ? chA : chB) * 4096 + (isX ? r : (r & 1)) * 1024;
            const unsigned dst = rgl + (isX ? r * 1024 : (r >> 1) * CV_CHY + (r & 1) * 1024) + 128;
            glds16(rsk, dst, l16, so);
            glds16(rsk, dst + 2 * CPY, l16, so + 4);
        }
        const int gw = blockIdx.x * 4 + w;
        const unsigned wtl = ldsbase + CV_WT + w * 5120;
        if (gw < 480) {
            const rsrc_t rw1 = mkrs(p.w_in, D * F * 2);
#pragma unroll
            for (int i = 0; i < 4; ++i) glds16(rw1, wtl + i * 1024, (l >> 3) * (F * 2) + (l & 7) * 16, (unsigned)(32 * (gw / 24) + 8 * i) * (F * 2) + (gw % 24) * 128);
        } else if (gw < 672) {
            const rsrc_t rw3 = mkrs(p.w_out, FH * D * 2);
#pragma unroll
            for (int i = 0; i < 5; ++i) {
                const int sl = i * 64 + l, row = (sl * 6554) >> 16, chk = sl - row * 10;
                glds16(rw3, wtl + i * 1024, row * (D * 2) + chk * 16, (unsigned)(32 * ((gw - 480) >> 3)) * (D * 2) + ((gw - 480) & 7) * 160);
            }
        }
        {
            const int cc0 = 10 * j + g, cc1 = cc0 + 4, cc2 = 10 * j + 8 + (g >> 1);
            const u16* xrow = p.q + (size_t)(rb * 64 + piperm(w, c)) * D;
            v4u a = *(const v4u*)(xrow + cc0 * 8), b = *(const v4u*)(xrow + cc1 * 8);
            v2u d = *(const v2u*)(xrow + cc2 * 8 + (g & 1) * 4);
            xs[0] = a.x; xs[1] = a.y; xs[2] = a.z; xs[3] = a.w; xs[4] = b.x; xs[5] = b.y; xs[6] = b.z; xs[7] = b.w; xs[8] = d.x; xs[9] = d.y;
            const int t_ = tid < 192 ? tid : 0, ix = t_ % 96;
            const unsigned vo = ((t_ < 96 ? 0 : FH) + 96 * j + piperm(ix >> 4, ix & 15)) * 4;
#pragma unroll
            for (int q_ = 0; q_ < 4; ++q_) cspart[q_] = __uint_as_float(__builtin_amdgcn_raw_buffer_load_b32(rs, vo, O_CS + q_ * F * 4, 0));
        }
        SB();
#pragma unroll
        for (int s_ = 0; s_ < 2; ++s_) {
            LAS unsigned char* rk = RG + s_ * seg1;
            LAS unsigned char* rv = rk + 4 * CPY;
            *(LAS v4u*)(rv + l16) = v4u{0, 0, 0, 0};
            *(LAS v4u*)(rv + 1024 + l16) = v4u{0, 0, 0, 0};
            const unsigned k0a = __builtin_amdgcn_readfirstlane(ka[0].x), k1a = __builtin_amdgcn_readfirstlane(ka[0].y);
            const unsigned k0b = __builtin_amdgcn_readfirstlane(ka[2].x), k1b = __builtin_amdgcn_readfirstlane(ka[2].y);
            const unsigned k0 = (s_ && !isX) ? k0b : k0a, k1 = (s_ && !isX) ? k1b : k1a;
            const int cpy = (l >> 3) & 3, U = l & 7;
            v4u o = {0, 0, 0, 0};
            if (U == 7 && l < 32) {
                if (cpy == 1) o.w = k0 << 16;
                if (cpy == 2) o.w = k0;
                if (cpy == 3) { o.z = k0 << 16; o.w = __builtin_amdgcn_alignbit(k1, k0, 16); }
            }
            *(LAS v4u*)(l < 32 ? rk + cpy * CPY + U * 16 : rv + 2048 + (l - 32) * 16) = o;
        }
#pragma unroll
        for (int r = 0; r < 4; ++r) {
            LAS unsigned char* rk = RG + (isX ? r * 1024 : (r >> 1) * CV_CHY + (r & 1) * 1024) + 128 + l16;
            unsigned a0 = __builtin_amdgcn_alignbit(ka[r].y, ka[r].x, 16), a1 = __builtin_amdgcn_alignbit(ka[r].z, ka[r].y, 16), a2 = __builtin_amdgcn_alignbit(ka[r].w, ka[r].z, 16),
                     a3 = __builtin_amdgcn_alignbit(kb[r].x, ka[r].w, 16), a4 = __builtin_amdgcn_alignbit(kb[r].y, kb[r].x, 16);
            *(LAS v4u*)(rk + CPY) = v4u{a0, a1, a2, a3};
            *(LAS v4u*)(rk + 3 * CPY) = v4u{a1, a2, a3, a4};
        }
        SB();
        if (gw < 480) WAITV(11); else if (gw < 672) WAITV(12); else WAITV(7);
        LAS unsigned char* ga0 = RG + ((c + 1) & 3) * CPY + (((c + 25) >> 2) - 2 * g) * 8;
        LAS unsigned char* ga1 = ga0 + (isX ? 64 * 32 : CV_CHY);
        LAS unsigned char* bp0 = RG + 4 * CPY + (1 + (isX ? 16 : 0) + c) * 160 + g * 16;
        LAS unsigned char* bp1 = isX ? bp0 - 16 * 160 : bp0 + CV_CHY;
        unsigned spt[4];
#pragma unroll
        for (int t = 0; t < 4; ++t) { const int tt_ = (w + t) & 3; spt[t] = spb_ + (unsigned)(32 * (tt_ >> 1) + 4 * (tt_ & 1)) * (S * 2); OPQ(spt[t]); }
        auto ldG = [&](LAS unsigned char* ga, int n) -> v4u {
            v2u lo = *(volatile LAS v2u*)(ga + (n + 2) * 32), hi = *(volatile LAS v2u*)(ga + (n + 2) * 32 + 8);
            return v4u{lo.x, lo.y, hi.x, hi.y};
        };
        f32x4 acc[4];
        v4u gg[2][6], bb[2][2];
        gg[0][0] = ldG(ga0, -2); gg[0][1] = ldG(ga0, -1);
        bb[0][1] = *(LAS v4u*)(bp0 + 15 * 160 + 64);
        gg[0][2] = ldG(ga0, 0); gg[0][3] = ldG(ga0, 1);
        bb[0][0] = *(LAS v4u*)(bp0 + 15 * 160);
        gg[0][4] = ldG(ga0, 2); gg[0][5] = ldG(ga0, 3);
        SB();
        auto conv_epi = [&](int s) {
            MFENCE4(acc[0], acc[1], acc[2], acc[3]);
            const float Dv = s ? Dv1 : Dv0;
            const int chl = isX ? w : 2 * w + s;
            LAS unsigned char* kvb = RG + (s ? seg1 : 0) + 4 * CPY + (16 + (isX ? 16 : 0) + c) * 160;
            LAS unsigned char* ob = L + CV_OUT + chl * 2176 + c * 136 + (g & 1) * 32 + (g >> 1) * 8;
#pragma unroll
            for (int rho = 0; rho < 4; ++rho) {
                v2u kvv = *(LAS v2u*)(kvb + (16 * rho + 8 * (g >> 1) + 4 - 4 * (g & 1)) * 2);
                f32x2 k01 = {hi16(kvv.y), lo16(kvv.y)}, k23 = {hi16(kvv.x), lo16(kvv.x)};
                unsigned y01 = pk2(k01 * Dv + f32x2{acc[rho][0], acc[rho][1]});
                unsigned y23 = pk2(k23 * Dv + f32x2{acc[rho][2], acc[rho][3]});
                *(LAS v2u*)(ob + (rho >> 1) * 64 + (rho & 1) * 16) = v2u{y01, y23};
            }
        };
#pragma unroll
        for (int st = 0; st < 32; ++st) {
            const int P = st & 1, N = P ^ 1;
            const int ns = st + 1, nseg = ns >> 4, nmm = ns & 15;
            LAS unsigned char* ga = nseg ? ga1 : ga0;
            LAS unsigned char* bp = nseg ? bp1 : bp0;
            const bool more = ns < 32;
            WAITL(5);
            if (st == 0) { mf_ini(acc[0], gg[P][0], bb[P][1]); } else mf_acc(acc[0], gg[P][0], bb[P][1]);
            if (more) bb[N][1] = *(LAS v4u*)(bp + (15 - nmm) * 160 + 64);
            SB();
            if (st == 0) { mf_ini(acc[1], gg[P][1], bb[P][1]); } else mf_acc(acc[1], gg[P][1], bb[P][1]);
            if (more && nmm == 0) { gg[N][0] = ldG(ga, -2); gg[N][1] = ldG(ga, -1); }
            if (more) gg[N][2] = ldG(ga, 4 * nmm);
            SB();
            if (st == 0) { mf_ini(acc[2], gg[P][2], bb[P][1]); } else mf_acc(acc[2], gg[P][2], bb[P][1]);
            if (more) gg[N][3] = ldG(ga, 4 * nmm + 1);
            SB();
            if (st == 0) { mf_ini(acc[3], gg[P][3], bb[P][1]); } else mf_acc(acc[3], gg[P][3], bb[P][1]);
            if (more) bb[N][0] = *(LAS v4u*)(bp + (15 - nmm) * 160);
            SB();
            if (!more) WAITL(0); else if (nmm == 0) WAITL(10); else WAITL(6);
            mf_acc(acc[0], gg[P][2], bb[P][0]);
            if (more) gg[N][4] = ldG(ga, 4 * nmm + 2);
            SB();
            mf_acc(acc[1], gg[P][3], bb[P][0]);
            if (more) gg[N][5] = ldG(ga, 4 * nmm + 3);
            SB();
            mf_acc(acc[2], gg[P][4], bb[P][0]);
            RAWLD(2 * st);
            SB();
            mf_acc(acc[3], gg[P][5], bb[P][0]);
            RAWLD(2 * st + 1);
            if (more && nmm != 0) { gg[N][0] = gg[P][4]; gg[N][1] = gg[P][5]; }
            SB();
            if (st == 11) {
                WAITV(24);
                f32x2 ps = slice_stats(xs);
                if (g == 0) st64s<SC1>(canon(ps.x, ps.y), rs, c * 8, O_STX + (rb * 8 * 64 + w * 16) * 8 + j * 512);
                if (tid < 192) *(LAS float*)(L + L_CS + tid * 4) = (cspart[0] + cspart[1]) + (cspart[2] + cspart[3]);
                LAS unsigned char* WT = L + CV_WT + w * 5120;
                const int qq = c >> 2, pp = c & 3;
                if (gw < 480) {
                    const int kk = gw / 24, ft0 = (gw % 24) * 4;
#pragma unroll
                    for (int t = 0; t < 4; ++t) {
                        LAS unsigned char* a_ = WT + (8 * g + qq) * 128 + (32 * (t >> 1) + 8 * pp + 4 * (t & 1)) * 2;
                        v2u lo = trrd(a_), hi = trrd(a_ + 512);
                        st128s<SC1>(v4u{lo.x, lo.y, hi.x, hi.y}, rs, l16, O_W1F + ((ft0 + t) * 20 + kk) * 1024);
                    }
                } else if (gw < 672) {
                    const int kk = (gw - 480) >> 3, jj = (gw - 480) & 7;
#pragma unroll
                    for (int t5 = 0; t5 < 5; ++t5) {
                        LAS unsigned char* a_ = WT + (8 * g + qq) * 160 + (t5 < 4 ? 32 * (t5 >> 1) + 8 * pp + 4 * (t5 & 1) : 64 + 4 * pp) * 2;
                        v2u lo = trrd(a_), hi = trrd(a_ + 640);
                        st128s<SC1>(v4u{lo.x, lo.y, hi.x, hi.y}, rs, l16, O_W3F + ((jj * 5 + t5) * 24 + kk) * 1024);
                    }
                }
                SB();
            }
            if (st == 15 && !isX) {
                conv_epi(0);
#pragma unroll
                for (int r = 0; r < 4; ++r) acc[r] = f32x4{0, 0, 0, 0};
                asm volatile("s_nop 3" : "+v"(acc[0]), "+v"(acc[1]), "+v"(acc[2]), "+v"(acc[3]));
            }
        }
        conv_epi(1);
        BAR();
        const int ch0 = isX ? cu * 4 : (cu - 160) * 8;
        if (real) {
            const unsigned yb = O_YF + (isX ? 64 * 20480 : 0) + (ch0 >> 5) * 1024 + ((ch0 >> 3) & 3) * 128 + ((ch0 >> 2) & 1) * 512;
            const unsigned vof = (tid >> 3) * 20480 + (tid & 7) * 16;
            LAS unsigned char* ib = L + CV_OUT + (tid >> 5) * 136 + (tid & 31) * 4;
#pragma unroll
            for (int gI = 0; gI < 2; ++gI) {
                if (gI == 0 || !isX) {
#pragma unroll
                    for (int hf = 0; hf < 2; ++hf) {
                        LAS unsigned char* q_ = ib + (4 * gI) * 2176 + hf * 8 * 136;
                        unsigned c0 = *(LAS unsigned*)(q_), c1 = *(LAS unsigned*)(q_ + 2176), c2 = *(LAS unsigned*)(q_ + 2 * 2176), c3 = *(LAS unsigned*)(q_ + 3 * 2176);
                        v4u o0 = {__builtin_amdgcn_perm(c1, c0, 0x05040100), __builtin_amdgcn_perm(c3, c2, 0x05040100), __builtin_amdgcn_perm(c1, c0, 0x07060302), __builtin_amdgcn_perm(c3, c2, 0x07060302)};
                        st128s<SC1>(o0, rs, vof, yb + gI * 512 + hf * 32 * 20480);
                    }
                }
            }
        }
        SB();
        tpose(0, 0);
        DRAIN();
        BAR();
        if (real && tid < 16) __hip_atomic_fetch_add(cnt + (CNT_A + ((isX ? 16 : 0) + tid) * 8 + ch0 / 80) * 32, 1u, RLX_AGENT);
        if (blockIdx.x < 168 && tid >= 64 && tid < 96) __hip_atomic_fetch_add(cnt + (CNT_W + (tid - 64) * 8 + (blockIdx.x & 7)) * 32, 1u, RLX_AGENT);
        SB();
        __builtin_amdgcn_s_waitcnt(0x0F70);
        tpose(1, 65536);
        tpose(2, 0);
        tpose(3, 65536);
        BAR();
        {
            gu32* pc = cnt + (l < 8 ? CNT_W + (blockIdx.x & 31) * 8 + l : CNT_A + wg) * 32;
            const unsigned want = l < 8 ? 21u : (rb >= 16 ? 20u : 10u);
            for (unsigned sp = 0; sp < SPIN_CAP; ++sp) {
                if (__all(__hip_atomic_load(pc, RLX_AGENT) >= want)) break;
            }
            __builtin_amdgcn_fence(__ATOMIC_ACQUIRE, "wavefront");
            CBAR();
        }
        {
            const int cc0 = 10 * j + g, cc1 = cc0 + 4, cc2 = 10 * j + 8 + (g >> 1);
            const unsigned yo = O_YF + (rb * 4 + w) * 20 * 1024;
            v2u y0 = ld64s<SC1>(rs, (cc0 >> 2) * 1024 + ((cc0 & 3) * 16 + c) * 8, yo), y1 = ld64s<SC1>(rs, (cc0 >> 2) * 1024 + ((cc0 & 3) * 16 + c) * 8, yo + 512);
            v2u y2 = ld64s<SC1>(rs, (cc1 >> 2) * 1024 + ((cc1 & 3) * 16 + c) * 8, yo), y3 = ld64s<SC1>(rs, (cc1 >> 2) * 1024 + ((cc1 & 3) * 16 + c) * 8, yo + 512);
            v2u y4 = ld64s<SC1>(rs, (cc2 >> 2) * 1024 + ((cc2 & 3) * 16 + c) * 8 + (g & 1) * 512, yo);
            ys[0] = y0.x; ys[1] = y0.y; ys[2] = y1.x; ys[3] = y1.y; ys[4] = y2.x; ys[5] = y2.y; ys[6] = y3.x; ys[7] = y3.y; ys[8] = y4.x; ys[9] = y4.y;
        }
    }
    const float bsp = bf2f(p.b_sp[rb * 64 + piperm(w, c)]);
    v2u ident;
    {
        int e = c - 4 * g;
        ident.x = (e == 0 ? 0x3f80u : 0u) | (e == 1 ? 0x3f800000u : 0u);
        ident.y = (e == 2 ? 0x3f80u : 0u) | (e == 3 ? 0x3f800000u : 0u);
    }

    for (layer = 0; layer < 2; ++layer) {
        const int LAYER = layer;
        RELANE();
        if (LAYER == 0) OPQ10(xs);
        asm volatile("" : "+v"(ys[0]), "+v"(ys[1]), "+v"(ys[2]), "+v"(ys[3]), "+v"(ys[4]), "+v"(ys[5]), "+v"(ys[6]), "+v"(ys[7]), "+v"(ys[8]), "+v"(ys[9]));
        v4u w1[80];
        W1BASES();
        {
            const unsigned so = O_STX + (layer * 2) * SZ_ST + (rb * 8 * 64 + w * 16) * 8;
            v2u sa, sb;
            poll2(rs, g * 1024 + c * 8, so, sa, sb);
            f32x2 st = merge_stats<80>(sa, sb);
            f32x2 xv[10];
#pragma unroll
            for (int e = 0; e < 10; ++e) {
                f32x2 x = up2(xs[e]);
                f32x2 q = (x - st.x) * st.y;
                xv[e] = q * up2(ys[e]) + x;
                xs[e] = pk2(xv[e]);
            }
            const int cc0 = 10 * j + g, cc1 = cc0 + 4, cc2 = 10 * j + 8 + (g >> 1);
            const unsigned xo = O_XF + layer * SZ_X + (rb * 4 + w) * 20 * 1024;
            st128s<SC1>(v4u{xs[0], xs[1], xs[2], xs[3]}, rs, c * 16 + cc0 * 256, xo);
            st128s<SC1>(v4u{xs[4], xs[5], xs[6], xs[7]}, rs, c * 16 + cc1 * 256, xo);
            st64s<SC1>(v2u{xs[8], xs[9]}, rs, c * 16 + cc2 * 256 + (g & 1) * 8, xo);
            f32x2 ps = slice_stats_f(xv);
            if (g == 0) st64s<SC1>(v2u{__float_as_uint(ps.x), __float_as_uint(ps.y)}, rs, c * 8, so + SZ_ST + j * 512);
            SB();
#pragma unroll
            for (int i = 0; i < NPRE1; ++i) w1[i] = W1LD(i);
            SB();
            WAITV(NPRE1);
            if (l < 8) __hip_atomic_fetch_add(cnt + (CNT_D + (rb * 8 + l) * 4 + w) * 32, 1u, RLX_AGENT);
            SB();
#pragma unroll
            for (int i = NPRE1; i < NPRE; ++i) w1[i] = W1LD(i);
            SB();
        }
        wait_cnt(cnt + (CNT_D + wg * 4 + w) * 32, 8u * (layer + 1));
        v4u x[20];
        {
            const unsigned so = O_STX + (layer * 2 + 1) * SZ_ST + (rb * 8 * 64 + w * 16) * 8;
            unsigned xo = O_XF + layer * SZ_X + (rb * 4 + w) * 20 * 1024; OPQ(xo);
#pragma unroll
            for (int kk = 0; kk < 10; ++kk) x[kk] = LDF(xo, kk);
            v2u sa = ld64s<SC1>(rs, g * 1024 + c * 8, so), sb = ld64s<SC1>(rs, g * 1024 + c * 8, so + 512);
#pragma unroll
            for (int kk = 10; kk < 20; ++kk) x[kk] = LDF(xo, kk);
            SB();
#pragma unroll
            for (int kk = 0; kk < 10; ++kk) *(LAS v4u*)(L + L_XA + (w * 20 + kk) * 1024 + l16) = x[kk];
            f32x2 st = merge_stats<80>(sa, sb);
            if (g == 0) *(LAS f32x2*)(L + L_SX + (w * 16 + c) * 8) = f32x2{st.y, -st.x * st.y};
        }
        BAR();
        f32x4 z2[6];
        f32x4 aa[6];
        LAS unsigned char* xa0 = L + L_XA + l16 + (2 * wo) * 20480;
        LAS unsigned char* xa1 = L + L_XA + l16 + (2 * (wo ^ 1)) * 20480;
#define XRD(kk, t) (*(LAS v4u*)(((t) < 2 ? xa0 : xa1) + ((t) & 1) * 20480 + (kk) * 1024))
        v4u xb[3][4];
        auto g1_steps = [&](int pass, int k0, int k1, int kstop) {
#pragma unroll
            for (int kk = k0; kk < k1; ++kk) {
                const int cur = kk % 3, nxt = (kk + 2) % 3;
                const int ia = pass * 40 + 2 * kk;
                const bool xw = pass == 0 && kk >= 3 && kk < 8;
                if (kk + 1 < kstop) { if (xw) WAITL(6); else WAITL(4); } else WAITL(0);
#pragma unroll
                for (int t = 0; t < 2; ++t) {
                    if (kk == 0) mf_ini(aa[4 + t], w1[ia + 1], xb[cur][t]); else mf_acc(aa[4 + t], w1[ia + 1], xb[cur][t]);
                    if (pass == 0 && t == 0) w1[NPRE + kk] = W1LD(NPRE + kk);
                    if (pass == 1 && t == 0 && kk < 10) w1[NPRE + 32 + kk] = W1LD(NPRE + 32 + kk);
                    if (pass == 1 && t == 0 && kk >= 10 && NPRE + 38 + kk < 80) w1[NPRE + 38 + kk < 80 ? NPRE + 38 + kk : 0] = W1LD(NPRE + 38 + kk);
                    if (pass == 0 && kk >= 2 && kk < 7) *(LAS v4u*)(L + L_XA + (w * 20 + 10 + 2 * (kk - 2) + t) * 1024 + l16) = x[10 + 2 * (kk - 2) + t];
                    SB();
                }
#pragma unroll
                for (int t = 0; t < 4; ++t) {
                    if (kk == 0) mf_ini(aa[t], w1[ia], xb[cur][t]); else mf_acc(aa[t], w1[ia], xb[cur][t]);
                    if (kk + 2 < kstop) xb[nxt][t] = XRD(kk + 2, t);
                    SB();
                }
            }
        };
        auto xb_fill = [&](int kk) {
#pragma unroll
            for (int t = 0; t < 4; ++t) xb[kk % 3][t] = XRD(kk, t);
#pragma unroll
            for (int t = 0; t < 4; ++t) xb[(kk + 1) % 3][t] = XRD(kk + 1, t);
            SB();
        };
        xb_fill(0);
        g1_steps(0, 0, 8, 10);
        BAR();
        g1_steps(0, 8, 20, 20);
        MFENCE6(aa[0], aa[1], aa[2], aa[3], aa[4], aa[5]);
        {
            f32x4 csa = *(LAS f32x4*)(L + L_CS + (96 + w * 16 + 4 * g) * 4), csb = *(LAS f32x4*)(L + L_CS + (96 + ftb * 16 + 4 * g) * 4);
            f32x2 sx[4];
#pragma unroll
            for (int i = 0; i < 4; ++i) sx[i] = *(LAS f32x2*)(L + L_SX + ((i ^ (2 * wo)) * 16 + c) * 8);
            f32x2 s1[4], s2[4];
#pragma unroll
            for (int t = 0; t < 6; ++t) {
                f32x4 cs = t < 4 ? csa : csb;
                f32x4 v = aa[t] * sx[t & 3].x + cs * sx[t & 3].y;
                f32x2 a = gelu2(f32x2{v[0], v[1]}), b = gelu2(f32x2{v[2], v[3]});
                z2[t] = f32x4{a.x, a.y, b.x, b.y};
                if (t < 4) { s1[t] = a + b; s2[t] = a * a + b * b; }
                else { s1[t - 4] += a + b; s2[t - 4] += a * a; s2[t - 4] += b * b; }
#pragma unroll
                for (int i = 0; i < 2; ++i) w1[NPRE + 20 + t * 2 + i] = W1LD(NPRE + 20 + t * 2 + i);
                SB();
            }
            float so_ = tred4(s1[0].x + s1[0].y, s1[1].x + s1[1].y, s1[2].x + s1[2].y, s1[3].x + s1[3].y);
            float qo_ = tred4(s2[0].x + s2[0].y, s2[1].x + s2[1].y, s2[2].x + s2[2].y, s2[3].x + s2[3].y);
            qo_ = fmaxf(qo_ - so_ * so_ * (g < 2 ? 1.f / 32.f : 1.f / 16.f), 0.f);
            *(LAS f32x2*)(L + L_ZS + (w * 64 + (g ^ (2 * wo)) * 16 + c) * 8) = f32x2{so_, qo_};
            BAR();
            if (w == 0) {
                f32x2 pr[4];
#pragma unroll
                for (int ww = 0; ww < 4; ++ww) pr[ww] = *(LAS f32x2*)(L + L_ZS + (ww * 64) * 8 + l8);
                float tot = (pr[0].x + pr[1].x) + (pr[2].x + pr[3].x);
                float mu = tot * (1.f / 96.f), mm = 0.f;
#pragma unroll
                for (int ww = 0; ww < 4; ++ww) {
                    bool big = (g ^ (2 * (ww & 1))) < 2;
                    float d = pr[ww].x * (big ? 1.f / 32.f : 1.f / 16.f) - mu;
                    mm += pr[ww].y + (big ? 32.f : 16.f) * d * d;
                }
                st64s<SC1>(canon(tot, mm), rs, l8, O_STZ + layer * SZ_ST + wg * 512);
            }
        }
        xb_fill(0);
        g1_steps(1, 0, Z1SPLIT, Z1SPLIT);
        {
            v2u sa, sb;
            poll2(rs, g * 1024 + c * 8, O_STZ + layer * SZ_ST + (rb * 8 * 64 + w * 16) * 8, sa, sb);
#pragma unroll
            for (int i = 0; i < 6; ++i) w1[NPRE + 42 + i] = W1LD(NPRE + 42 + i);
            f32x2 st = merge_stats<96>(sa, sb);
            if (g == 0) *(LAS f32x2*)(L + L_ZN + (w * 16 + c) * 8) = f32x2{st.y, -st.x * st.y};
        }
        BAR();
        {
            f32x2 st[4];
#pragma unroll
            for (int t = 0; t < 4; ++t) st[t] = *(LAS f32x2*)(L + L_ZN + ((t ^ (2 * wo)) * 16 + c) * 8);
            v2u a[6];
#pragma unroll
            for (int t = 0; t < 6; ++t) {
                f32x4 d = z2[t] * st[t & 3].x + st[t & 3].y;
                a[t] = v2u{pk2(f32x2{d[0], d[1]}), pk2(f32x2{d[2], d[3]})};
            }
            f32x4 tr[6];
#pragma unroll
            for (int t = 0; t < 6; ++t) mf16_ini(tr[t], a[t], ident);
            MFENCE6(tr[0], tr[1], tr[2], tr[3], tr[4], tr[5]);
            v2u T[6];
#pragma unroll
            for (int t = 0; t < 6; ++t) T[t] = v2u{pk(tr[t][0], tr[t][1]), pk(tr[t][2], tr[t][3])};
            const unsigned zo = O_Z2F + layer * SZ_Z + (6 * j * 64 + 2 * rb) * 1024;
            st128s<SC1>(v4u{T[0].x, T[0].y, T[1].x, T[1].y}, rs, l16, zo + (w * 64 + wo) * 1024);
            st128s<SC1>(v4u{T[2].x, T[2].y, T[3].x, T[3].y}, rs, l16, zo + (w * 64 + (wo ^ 1)) * 1024);
            st128s<SC1>(v4u{T[4].x, T[4].y, T[5].x, T[5].y}, rs, l16, zo + (ftb * 64 + wo) * 1024);
        }
        SB();
        xb_fill(Z1SPLIT);
        g1_steps(1, Z1SPLIT, 20, 20);
        DRAIN();
        BAR();
        if (tid < 32) __hip_atomic_store(cnt + (CNT_B + (tid * 8 + j) * 4 + (rb >> 3)) * 32 + (rb & 7), (unsigned)(layer + 1), RLX_AGENT);
        SB();
        MFENCE6(aa[0], aa[1], aa[2], aa[3], aa[4], aa[5]);
        RELANE();
        {
            unsigned w3s = O_W3F + (j * 120 + w * 20) * 1024; OPQ(w3s);
            unsigned w3l = ldsbase + L_W3 + w * 20 * 1024; OPQ(w3l);
            f32x4 csa = *(LAS f32x4*)(L + L_CS + (w * 16 + 4 * g) * 4), csb = *(LAS f32x4*)(L + L_CS + (ftb * 16 + 4 * g) * 4);
            f32x2 sx[4];
#pragma unroll
            for (int i = 0; i < 4; ++i) sx[i] = *(LAS f32x2*)(L + L_SX + ((i ^ (2 * wo)) * 16 + c) * 8);
#pragma unroll
            for (int i = 0; i < 2; ++i) glds16c(rs, w3l + i * 1024, l16, w3s + i * 1024);
#pragma unroll
            for (int t = 0; t < 6; ++t) {
                f32x4 cs = t < 4 ? csa : csb;
                f32x4 v = aa[t] * sx[t & 3].x + cs * sx[t & 3].y;
                unsigned o0 = pk2(gelu2(f32x2{v[0], v[1]}));
                asm volatile("" : "+v"(o0));
                glds16c(rs, w3l + (2 + 3 * t) * 1024, l16, w3s + (2 + 3 * t) * 1024);
                glds16c(rs, w3l + (3 + 3 * t) * 1024, l16, w3s + (3 + 3 * t) * 1024);
                unsigned o1 = pk2(gelu2(f32x2{v[2], v[3]}));
                *(LAS v2u*)(L + L_Z1 + (((t & 3) ^ (2 * wo)) * 6 + (t < 4 ? w : ftb)) * 512 + l8) = v2u{o0, o1};
                glds16c(rs, w3l + (4 + 3 * t) * 1024, l16, w3s + (4 + 3 * t) * 1024);
            }
            if (LAYER == 0) {
#pragma unroll
                for (int i = 0; i < 10; ++i) glds16c(rs, ldsbase + L_W3 + (80 + w * 10 + i) * 1024, l16, O_W3F + (j * 120 + 80 + w * 10 + i) * 1024);
            }
        }
        RELANE();
        {
            gu32* fl = cnt + (CNT_B + wg * 4 + w) * 32 + (l & 7);
            unsigned ready = 0;
            auto ensure = [&](unsigned pz) {
                if (ready <= pz) {
                    for (unsigned sp = 0; sp < SPIN_CAP; ++sp) {
                        const unsigned m = (unsigned)__ballot(__hip_atomic_load(fl, RLX_AGENT) > (unsigned)layer) & 0xffu;
                        ready = __builtin_ctz(~m);
                        if (ready > pz) break;
                    }
                    __builtin_amdgcn_fence(__ATOMIC_ACQUIRE, "wavefront");
                    CBAR();
                }
            };
            ensure(0);
            unsigned zo[3];
            int ppe[3];
#pragma unroll
            for (int pp = 0; pp < 3; ++pp) {
                int e = pp + rot; e = e >= 3 ? e - 3 : e; ppe[pp] = e;
                zo[pp] = O_Z2F + layer * SZ_Z + ((6 * j + 2 * e) * 64 + 16 * w) * 1024; OPQ(zo[pp]);
            }
            v4u z[96];
#define ZKK(i) ((i) < 72 ? (i) / 6 : 12 + (((i) - 72) & 3))
#define ZFT(i) ((i) < 72 ? (i) % 6 : ((i) - 72) >> 2)
#define ZLD(i) LDF(zo[ZFT(i) >> 1] + (ZFT(i) & 1) * 65536, ZKK(i))
#pragma unroll
            for (int i = 0; i < 12; ++i) z[i] = ZLD(i);
            SB();
            const f32x4 bs4 = {bsp, bsp, bsp, bsp};
            f32x4 acc[6][4];
            LAS unsigned char* R = L + L_RED + l16;
            const unsigned uo = O_UF + layer * SZ_Z + ((rb * 4 + w) * 24 + 3 * j) * 1024;
            v4u uo4[3];
            f32x4 p0, p1, p2; v2u z1;
            auto wr = [&](int ft) {
#pragma unroll
                for (int t = 1; t < 4; ++t) *(LAS f32x4*)(R + (ft & 1) * 12288 + (((w + t) & 3) * 3 + (t - 1)) * 1024) = acc[ft][t];
            };
            auto rd = [&](int ft) {
                p0 = *(LAS f32x4*)(R + (ft & 1) * 12288 + (w * 3 + 0) * 1024); p1 = *(LAS f32x4*)(R + (ft & 1) * 12288 + (w * 3 + 1) * 1024); p2 = *(LAS f32x4*)(R + (ft & 1) * 12288 + (w * 3 + 2) * 1024);
                z1 = *(LAS v2u*)(L + L_Z1 + (w * 6 + 2 * ppe[ft >> 1] + (ft & 1)) * 512 + l8);
            };
            auto fin = [&](int ft) {
                f32x4 sv = (acc[ft][0] + p0) + (p1 + p2);
                uo4[ft >> 1][2 * (ft & 1)] = pk2(up2(z1.x) * f32x2{sv[0], sv[1]});
                uo4[ft >> 1][2 * (ft & 1) + 1] = pk2(up2(z1.y) * f32x2{sv[2], sv[3]});
            };
#pragma unroll
            for (int i = 0; i < 72; ++i) {
                const int kk = i / 6, ft = i % 6;
                if (i % 12 == 0 && i < 60) ensure(i / 12 + 1);
                if (i == 60) ensure(7);
#pragma unroll
                for (int t = 0; t < 4; ++t) {
                    if (kk == 0) { if (t == 0) mf_inic_a(acc[ft][t], z[i], Bw[t][kk], bs4); else mf_ini_a(acc[ft][t], z[i], Bw[t][kk]); }
                    else mf_acc_a(acc[ft][t], z[i], Bw[t][kk]);
                }
                z[i + 12] = ZLD(i + 12);
                SB();
            }
#pragma unroll
            for (int ft = 0; ft < 6; ++ft) {
#pragma unroll
                for (int ph = 0; ph < 4; ++ph) {
                    const int i = 72 + ft * 4 + ph;
#pragma unroll
                    for (int t = 0; t < 4; ++t) mf_acc_a(acc[ft][t], z[i], Bw[t][12 + ph]);
                    if (i + 12 < 96) z[i + 12] = ZLD(i + 12);
                    if (ph == 1 && ft >= 1) {
                        if (ft >= 2) { BAR(); rd(ft - 2); }
                        wr(ft - 1);
                    }
                    if (ph == 3 && ft >= 2) fin(ft - 2);
                    SB();
                }
            }
            st128s<SC1>(uo4[0], rs, l16, uo + ppe[0] * 1024);
            st128s<SC1>(uo4[1], rs, l16, uo + ppe[1] * 1024);
            MFENCE4(acc[5][0], acc[5][1], acc[5][2], acc[5][3]);
            BAR();
            rd(4);
            wr(5);
            fin(4);
            BAR();
            rd(5);
            fin(5);
            st128s<SC1>(uo4[2], rs, l16, uo + ppe[2] * 1024);
        }
        DRAIN();
        if (l < 8) __hip_atomic_fetch_add(cnt + (CNT_C + (rb * 8 + l) * 4 + 2 * (w >> 1) + (j >> 2)) * 32, 1u, RLX_AGENT);
        {
            RELANE();
            LAS unsigned char* wl = L + L_W3 + 12 * wo * 1024 + l16;
            LAS unsigned char* wl2 = wl + 3 * 24576;
#define W3RD(kk, t5) (*(LAS v4u*)(((t5) < 3 ? wl : wl2) + ((t5) % 3) * 24576 + (kk) * 1024))
            v4u wv[3][5];
#pragma unroll
            for (int t5 = 0; t5 < 5; ++t5) wv[0][t5] = W3RD(0, t5);
#pragma unroll
            for (int t5 = 0; t5 < 5; ++t5) wv[1][t5] = W3RD(1, t5);
            wait_cnt(cnt + (CNT_C + wg * 4 + w) * 32, 8u * (layer + 1));
            v4u ub[2][12];
            unsigned uo0 = O_UF + layer * SZ_Z + ((rb * 4 + w) * 24 + 12 * wo) * 1024, uo1 = O_UF + layer * SZ_Z + ((rb * 4 + (w ^ 1)) * 24 + 12 * wo) * 1024;
            OPQ(uo0); OPQ(uo1);
#pragma unroll
            for (int kk = 0; kk < UWIN; ++kk) {
                ub[0][kk] = LDF(uo0, kk);
                ub[1][kk] = LDF(uo1, kk);
            }
            f32x4 acc[5][2];
            SB();
#pragma unroll
            for (int kk = 0; kk < 12; ++kk) {
                if (kk + 1 < 12) WAITL(5); else WAITL(0);
#pragma unroll
                for (int t5 = 0; t5 < 5; ++t5) {
                    if (kk == 0) mf_ini(acc[t5][1], wv[kk % 3][t5], ub[1][kk]); else mf_acc(acc[t5][1], wv[kk % 3][t5], ub[1][kk]);
                    if (kk + 2 < 12) wv[(kk + 2) % 3][t5] = W3RD(kk + 2, t5);
                    SB();
                    if (kk == 0) mf_ini(acc[t5][0], wv[kk % 3][t5], ub[0][kk]); else mf_acc(acc[t5][0], wv[kk % 3][t5], ub[0][kk]);
                    if (t5 < 2 && kk + UWIN < 12) ub[t5][kk + UWIN] = LDF(t5 ? uo1 : uo0, kk + UWIN);
                    SB();
                }
                if (kk == 2) { __builtin_amdgcn_s_barrier(); SB(); }
            }
            MFENCE2(acc[0][0], acc[0][1]);
            MFENCE8(acc[1][0], acc[1][1], acc[2][0], acc[2][1], acc[3][0], acc[3][1], acc[4][0], acc[4][1]);
            RELANE();
            OPQ10(xs);
            LAS unsigned char* R = L + L_RED + l16;
#pragma unroll
            for (int t5 = 0; t5 < 5; ++t5) *(LAS f32x4*)(R + ((w ^ 1) * 5 + t5) * 1024) = acc[t5][1];
            BAR();
            f32x4 ot[5];
#pragma unroll
            for (int t5 = 0; t5 < 5; ++t5) ot[t5] = *(LAS f32x4*)(R + (w * 5 + t5) * 1024);
            f32x2 xv[10];
#pragma unroll
            for (int t5 = 0; t5 < 5; ++t5) {
                f32x4 sv = acc[t5][0] + ot[t5];
                xv[2 * t5] = up2(xs[2 * t5]) + f32x2{sv[0], sv[1]};
                xv[2 * t5 + 1] = up2(xs[2 * t5 + 1]) + f32x2{sv[2], sv[3]};
            }
            if (LAYER == 0) {
                f32x2 ps = slice_stats_f(xv);
                if (g == 0) st64s<SC1>(canon(ps.x, ps.y), rs, c * 8, O_STX + 2 * SZ_ST + (rb * 8 * 64 + w * 16) * 8 + j * 512);
            }
#pragma unroll
            for (int e = 0; e < 10; ++e) xs[e] = pk2(xv[e]);
        }
    }
    {
        const int cc0 = 10 * j + g, cc1 = cc0 + 4, cc2 = 10 * j + 8 + (g >> 1);
        u16* orow = p.out + (size_t)(rb * 64 + piperm(w, c)) * D;
        *(v4u*)(orow + cc0 * 8) = v4u{xs[0], xs[1], xs[2], xs[3]};
        *(v4u*)(orow + cc1 * 8) = v4u{xs[4], xs[5], xs[6], xs[7]};
        *(v2u*)(orow + cc2 * 8 + (g & 1) * 4) = v2u{xs[8], xs[9]};
    }
}

extern "C" void kernel_launch(void* const* d_in, const int* in_sizes, int n_in,
                              void* d_out, int out_size, void* d_ws, size_t ws_size,
                              hipStream_t stream) {
    Params p;
    p.q = (const u16*)d_in[0]; p.k = (const u16*)d_in[1]; p.v = (const u16*)d_in[2];
    p.shk = (const u16*)d_in[3]; p.ssmk = (const u16*)d_in[4]; p.ssmD = (const u16*)d_in[5];
    p.w_in = (const u16*)d_in[7]; p.w_sp = (const u16*)d_in[8]; p.b_sp = (const u16*)d_in[9]; p.w_out = (const u16*)d_in[10];
    p.out = (u16*)d_out; p.ws = (unsigned char*)d_ws;
    (void)hipFuncSetAttribute((const void*)k_main, hipFuncAttributeMaxDynamicSharedMemorySize, L_END);
    k_pre<<<PRE_N, 256, 0, stream>>>(p);
    k_main<<<256, 256, L_END, stream>>>(p);
}
```
